```python
import torch

def _softmax(x, axis=-1):
    return torch.softmax(x, dim=axis)

def _gelu(x):
    return torch.nn.functional.gelu(x, approximate='tanh')

def _silu(x):
    return torch.nn.functional.silu(x)

def _sigmoid(x):
    return torch.sigmoid(x)

def _layernorm(x, axis=-1, eps=1e-05):
    m = x.mean(dim=axis, keepdim=True)
    v = x.var(dim=axis, keepdim=True, unbiased=False)
    return (x - m) / torch.sqrt(v + eps)

def _scatter_add(tgt, idx, src, axis=0):
    return tgt.clone().index_add_(axis, idx, src)

def _segment_sum(src, seg, n):
    out = torch.zeros((n,) + src.shape[1:], dtype=src.dtype)
    return out.index_add_(0, seg, src)

def _pad(x, pw):
    flat = tuple((v for pair in reversed(pw) for v in pair))
    return torch.nn.functional.pad(x, flat)
kernel_name = 'sequential_preLN__memory_aug_recurrent_mem_tr__mixer_token_mlp__prompt'

def setup_inputs():

    _w = lambda a: (lambda t: t.to(torch.bfloat16) if t.is_floating_point() else t)(torch.as_tensor(a))

    def _A_setup():
        B, S, Dm = (1, 2048, 512)
        H, Dh = (32, 128)
        M, seg = (8, 32)
        seg = min(seg, S)
        S = S // seg * seg
        return {'x': _w((lambda *s: torch.randn(*s, dtype=torch.bfloat16) * 0.05)(B, S, Dm)), 'mem0': _w((lambda *s: torch.randn(*s, dtype=torch.bfloat16) * 0.2)(B, M, Dm)), 'Wq': _w((lambda *s: torch.randn(*s, dtype=torch.bfloat16) * 1.6)(Dm, H, Dh) * Dm ** (-0.5)), 'Wk': _w((lambda *s: torch.randn(*s, dtype=torch.bfloat16) * 1.6)(Dm, H, Dh) * Dm ** (-0.5)), 'Wv': _w((lambda *s: torch.randn(*s, dtype=torch.bfloat16) * 0.2)(Dm, H, Dh) * Dm ** (-0.5)), 'Wo': _w((lambda *s: torch.randn(*s, dtype=torch.bfloat16) * 0.2)(H, Dh, Dm) * (H * Dh) ** (-0.5)), 'seg_len': seg}
    _A_in = _A_setup()

    def _B_setup():
        B, S, D, F = (1, 2048, 512, 1792)
        return {'x': _w((lambda *s: torch.randn(*s, dtype=torch.bfloat16) * 0.05)(B, S, D)), 'w1': _w((lambda *s: torch.randn(*s, dtype=torch.bfloat16) * 0.4)(S, F) * S ** (-0.5)), 'b1': _w((lambda *s: torch.randn(*s, dtype=torch.bfloat16) * 3.2)(F) * 0.02), 'w2': _w((lambda *s: torch.randn(*s, dtype=torch.bfloat16) * 0.2)(F, S) * F ** (-0.5)), 'b2': _w((lambda *s: torch.randn(*s, dtype=torch.bfloat16) * 0.4)(S) * 0.02)}
    _B_in = _B_setup()
    return {'A_x': _A_in['x'], 'A_mem0': _A_in['mem0'], 'A_Wq': _A_in['Wq'], 'A_Wk': _A_in['Wk'], 'A_Wv': _A_in['Wv'], 'A_Wo': _A_in['Wo'], 'A_seg_len': _A_in['seg_len'], 'B_x': _B_in['x'], 'B_w1': _B_in['w1'], 'B_b1': _B_in['b1'], 'B_w2': _B_in['w2'], 'B_b2': _B_in['b2']}

def reference(A_x, A_mem0, A_Wq, A_Wk, A_Wv, A_Wo, A_seg_len, B_x, B_w1, B_b1, B_w2, B_b2):

    def _A_ref(x, mem0, Wq, Wk, Wv, Wo, seg_len):
        B, S, Dm = x.shape
        H, Dh = (Wq.shape[1], Wq.shape[2])
        M = mem0.shape[1]
        scale = Dh ** (-0.5)
        n_seg = S // seg_len
        L = M + seg_len
        idx = torch.arange(L)
        mask = (idx[:, None] < M) | (idx[:, None] >= idx[None, :])
        mem = mem0
        outs = []
        for s in range(n_seg):
            xs = x[:, s * seg_len:(s + 1) * seg_len, :].clone()
            h = torch.concatenate([mem, xs], axis=1)
            q = torch.einsum('btd,dhe->bhte', h, Wq)
            k = torch.einsum('btd,dhe->bhte', h, Wk)
            v = torch.einsum('btd,dhe->bhte', h, Wv)
            scores = torch.einsum('bhqe,bhke->bhqk', q, k) * scale
            scores = torch.where(mask[None, None], scores, -1000000000.0)
            p = _softmax(scores, axis=-1)
            o = torch.einsum('bhqk,bhke->bhqe', p, v)
            o = torch.einsum('bhte,hed->btd', o, Wo)
            mem = o[:, :M, :].clone()
            outs.append(o[:, M:, :])
        return torch.concatenate(outs, axis=1)

    def _B_ref(x, w1, b1, w2, b2):
        B, S, D = x.shape
        h = _layernorm(x, axis=-1)
        u = torch.einsum('bsd,sf->bdf', h, w1) + b1
        u = _gelu(u)
        y = torch.einsum('bdf,fs->bds', u, w2) + b2
        return (lambda x, *d: x.permute(*d))(y, 0, 2, 1)
    x = A_x
    h = x + _A_ref(_layernorm(x, axis=-1), A_mem0, A_Wq, A_Wk, A_Wv, A_Wo, A_seg_len)
    h2 = h + _B_ref(_layernorm(h, axis=-1), B_w1, B_b1, B_w2, B_b2)
    return h2
```

```cpp
#include <hip/hip_runtime.h>
#include <hip/hip_bf16.h>
#include <stdint.h>
#include <type_traits>

#define GAS __attribute__((address_space(1)))
#define LAS __attribute__((address_space(3)))
typedef __bf16 bf16x2 __attribute__((ext_vector_type(2)));
typedef __bf16 bf16x4 __attribute__((ext_vector_type(4)));
typedef __bf16 bf16x8 __attribute__((ext_vector_type(8)));
typedef short s16x4 __attribute__((ext_vector_type(4)));
typedef float f32x2 __attribute__((ext_vector_type(2)));
typedef float f32x4 __attribute__((ext_vector_type(4)));
typedef unsigned u32x2 __attribute__((ext_vector_type(2)));
typedef unsigned u32x4 __attribute__((ext_vector_type(4)));
typedef __amdgpu_buffer_rsrc_t rsrc_t;
typedef unsigned short u16;
typedef GAS unsigned gu32;

constexpr int S_ = 2048, D_ = 512, H_ = 32, E_ = 128, NSEG = 64, F_ = 1792;
constexpr int AUX_SC1 = 16;
#define RLX_AGENT __ATOMIC_RELAXED, __HIP_MEMORY_SCOPE_AGENT

__device__ __forceinline__ float u2f(unsigned x) { return __builtin_bit_cast(float, x); }
__device__ __forceinline__ unsigned f2u(float x) { return __builtin_bit_cast(unsigned, x); }
__device__ __forceinline__ float bflo(unsigned x) { return u2f(x << 16); }
__device__ __forceinline__ float bfhi(unsigned x) { return u2f(x & 0xffff0000u); }
__device__ __forceinline__ unsigned pk_bf16(float lo, float hi) {
  f32x2 f = {lo, hi};
  bf16x2 b = __builtin_convertvector(f, bf16x2);
  return __builtin_bit_cast(unsigned, b);
}
__device__ __forceinline__ u32x4 pk8(f32x4 a, f32x4 b) { return u32x4{pk_bf16(a[0], a[1]), pk_bf16(a[2], a[3]), pk_bf16(b[0], b[1]), pk_bf16(b[2], b[3])}; }
__device__ __forceinline__ f32x4 mfma32(u32x4 a, u32x4 b, f32x4 c) {
  return __builtin_amdgcn_mfma_f32_16x16x32_bf16(__builtin_bit_cast(bf16x8, a), __builtin_bit_cast(bf16x8, b), c, 0, 0, 0);
}
__device__ __forceinline__ u32x2 lds_tr(LAS unsigned char* p) {
  bf16x4 v = __builtin_amdgcn_ds_read_tr16_b64_v4bf16((LAS bf16x4*)p);
  return __builtin_bit_cast(u32x2, v);
}
__device__ __forceinline__ u32x4 cat(u32x2 a, u32x2 b) { return u32x4{a.x, a.y, b.x, b.y}; }
__device__ __forceinline__ float xlane16_max(float v) { unsigned x = f2u(v); u32x2 r = __builtin_amdgcn_permlane16_swap(x, x, false, false); unsigned a = r.x, b = r.y; return fmaxf(u2f(a), u2f(b)); }
__device__ __forceinline__ float xlane32_max(float v) { unsigned x = f2u(v); u32x2 r = __builtin_amdgcn_permlane32_swap(x, x, false, false); unsigned a = r.x, b = r.y; return fmaxf(u2f(a), u2f(b)); }
typedef u32x4 desc_t;
__device__ __forceinline__ desc_t mk_desc(const void* p, unsigned bytes) {
  const size_t a = (size_t)p;
  return desc_t{(unsigned)a, (unsigned)(a >> 32) & 0xffffu, bytes, 0x00020000u};
}
template <bool SC1, int OFF = 0> __device__ __forceinline__ void dma16(desc_t rs, unsigned voff, unsigned soff, unsigned lds_dst) {
  if (SC1) asm volatile("s_mov_b32 m0, %3\n\ts_nop 0\n\tbuffer_load_dwordx4 %0, %1, %2 offen offset:%c4 sc1 lds" :: "v"(voff), "s"(rs), "s"(soff), "s"(lds_dst), "i"(OFF) : "memory");
  else asm volatile("s_mov_b32 m0, %3\n\ts_nop 0\n\tbuffer_load_dwordx4 %0, %1, %2 offen offset:%c4 lds" :: "v"(voff), "s"(rs), "s"(soff), "s"(lds_dst), "i"(OFF) : "memory");
}
template <bool SC1> __device__ __forceinline__ void dma16q(int q, desc_t rs, unsigned voff, unsigned soff, unsigned lds_dst) {
  if (q == 0) dma16<SC1, 0>(rs, voff, soff, lds_dst); else if (q == 1) dma16<SC1, 1024>(rs, voff, soff, lds_dst);
  else if (q == 2) dma16<SC1, 2048>(rs, voff, soff, lds_dst); else dma16<SC1, 3072>(rs, voff, soff, lds_dst);
}
__device__ __forceinline__ unsigned pin_s(unsigned x) { asm volatile("" : "+s"(x)); return x; }
#define WAIT_V(n) asm volatile("s_waitcnt vmcnt(" #n ")" ::: "memory")
#define WAIT_L(n) asm volatile("s_waitcnt lgkmcnt(" #n ")" ::: "memory")
#define BAR() do { asm volatile("" ::: "memory"); __builtin_amdgcn_s_barrier(); asm volatile("" ::: "memory"); } while (0)
__device__ __forceinline__ rsrc_t mk_rsrc(const void* p, unsigned bytes) { return __builtin_amdgcn_make_buffer_rsrc((void*)p, 0, bytes, 0x00020000); }
__device__ __forceinline__ u32x4 ld_sc1(rsrc_t rs, unsigned voff, unsigned soff) { return __builtin_amdgcn_raw_buffer_load_b128(rs, voff, soff, AUX_SC1); }
__device__ __forceinline__ void st_sc1(u32x4 v, rsrc_t rs, unsigned voff, unsigned soff) { __builtin_amdgcn_raw_buffer_store_b128(v, rs, voff + soff, 0, AUX_SC1); }
constexpr unsigned MEMX_OFF = 0x0000000, STMP_OFF = 0x0100000, PART_OFF = 0x0200000, XCH_BYTES = 0x0400000;
constexpr unsigned PROBE_OFF = 0x0400000, FLAG_OFF = 0x0440000, FLAG_SPAN = 0x1C000, EPOCH_OFF = 0x0401000, INIT_OFF = 0x0402000;
constexpr unsigned XN_OFF = 0x0600000, HB_OFF = 0x0800000, HN_OFF = 0x0C00000, G_OFF = 0x1000000;
constexpr unsigned KF_OFF = 0x2000000, VF_OFF = 0x4000000, QF_OFF = 0x6000000, KVM_OFF = 0x8000000, PO_OFF = 0xA000000;
constexpr unsigned WS_END = 0xE000000;
constexpr unsigned SENT = 0xFFA5FFA5u;
constexpr int FL_SEL = 0, FL_TMO = 1, FL_K = 8  , FL_V = 160  , FL_Q = 40  , FL_KM = 64  , FL_AO = 128  , FL_R = 192  , FL_G = 256  , FL_I = 320  , FL_X = 384  , FL_AC = 400  ;
constexpr unsigned CODE_LEN = 71680;
constexpr int SEG3 = 54;
constexpr int LDS_BYTES = 24576 + 131072;
constexpr unsigned SPIN_CAP = 1u << 18;

struct Params {
  const u16* x; const u16* mem0; const u16* Wq; const u16* Wk; const u16* Wv; const u16* Wo;
  const u16* w1; const u16* b1; const u16* w2; const u16* b2;
  u16* out; unsigned char* ws; unsigned long long* dbg;
  int stg, ca, cr, stg2, cs, roles;
  unsigned fsel, pad;
};
typedef GAS unsigned long long gu64;
__device__ __forceinline__ unsigned boot_wait(const Params& p, unsigned long long tag) {
  const int lane = threadIdx.x & 63;
  gu64* f = (gu64*)(p.ws + PROBE_OFF + (lane & 7) * 128);
  unsigned long long v;
  for (unsigned spins = 0;; ++spins) {
    v = __hip_atomic_load(f, RLX_AGENT);
    if (__all((v >> 16) == tag) || spins > (1u << 20)) break;
    __builtin_amdgcn_s_sleep(2);
  }
  __builtin_amdgcn_fence(__ATOMIC_ACQUIRE, "wavefront");
  const unsigned l = (unsigned)v & 0xffffu;
  const unsigned l0 = __builtin_amdgcn_readlane(l, 0), l1 = __builtin_amdgcn_readlane(l, 1), l2 = __builtin_amdgcn_readlane(l, 2), l3 = __builtin_amdgcn_readlane(l, 3);
  int d1 = (int)l0 - (int)l1, d2 = (int)l3 - (int)l2;
  if (min(abs(d1), abs(d2)) < 150) d1 = d2 = d1 + d2;
  return __builtin_amdgcn_readfirstlane((d1 > 0 ? 1u : 0u) | (d2 > 0 ? 0u : 2u));
}
__device__ __forceinline__ unsigned spread(const Params& p, unsigned base, unsigned o) {
  const unsigned x = base + ((o >> 13) << 14) + (o & 8191u);
  return x | (((p.fsel >> ((x >> 21) & 1u)) & 1u) << 13);
}

__device__ __forceinline__ gu32* flagp(const Params& p, int idx) { return (gu32*)(p.ws + (spread(p, FLAG_OFF, (unsigned)idx * 128u) ^ ((idx >> 6) == 1 ? 0x2000u : 0u))); }
__device__ __forceinline__ void wait_prog(const Params& p, int idx, unsigned want, unsigned& v, int nap) {
  if (__all(idx < 0 || v >= want)) return;
  gu32* f = flagp(p, idx < 0 ? FL_TMO : idx);
  for (unsigned spins = 0;; ++spins) {
    v = __hip_atomic_load(f, RLX_AGENT);
    if (__all(idx < 0 || v >= want)) break;
    if (spins > SPIN_CAP) { __hip_atomic_store(flagp(p, FL_TMO), 1u, RLX_AGENT); break; }
    if ((spins & 63u) == 63u && __builtin_amdgcn_readfirstlane(__hip_atomic_load(flagp(p, FL_TMO), RLX_AGENT))) break;
    if (nap == 1) __builtin_amdgcn_s_sleep(1); else if (nap == 4) __builtin_amdgcn_s_sleep(4); else __builtin_amdgcn_s_sleep(100);
  }
  __builtin_amdgcn_fence(__ATOMIC_ACQUIRE, "wavefront");
}
#define TS(k) do {} while (0)
#define TSK(base, k) do {} while (0)
#define CYC(n) do {} while (0)
#define ACCDECL do {} while (0)
#define ACC(k, v) do {} while (0)
#define ACCOUT do {} while (0)
#define CHAIN_ASM \
"s_memrealtime s[70:71]\n" \
"s_mov_b64 s[36:37], %5\n" \
"s_and_b32 s37, s37, 0xffff\n" \
"s_mov_b32 s38, 0xe000000\n" \
"s_mov_b32 s39, 0x20000\n" \
"s_mov_b32 s66, %6\n" \
"s_mov_b32 s63, %7\n" \
"s_mov_b32 s60, %8\n" \
"s_mov_b32 s61, %9\n" \
"s_mov_b64 s[76:77], %10\n" \
"s_lshr_b32 s64, s66, 1\n" \
"s_and_b32 s65, s66, 1\n" \
"v_mbcnt_lo_u32_b32 v32, -1, 0\n" \
"v_mbcnt_hi_u32_b32 v32, -1, v32\n" \
"v_lshrrev_b32 v33, 4, v32\n" \
"v_and_b32 v34, 15, v32\n" \
"v_bfe_u32 v35, v32, 2, 2\n" \
"v_and_b32 v36, 3, v32\n" \
"v_lshlrev_b32 v0, 4, v32\n" \
"v_and_b32 v37, 1, v33\n" \
"v_lshlrev_b32 v38, 1, v33\n" \
"v_xor_b32 v38, v38, v34\n" \
"v_xor_b32 v39, 0, v38\n" \
"v_lshlrev_b32 v39, 4, v39\n" \
"v_lshl_add_u32 v40, v33, 13, v39\n" \
"v_xor_b32 v39, 0, v38\n" \
"v_lshlrev_b32 v39, 4, v39\n" \
"v_lshl_add_u32 v41, v33, 13, v39\n" \
"v_xor_b32 v39, 8, v38\n" \
"v_lshlrev_b32 v39, 4, v39\n" \
"v_lshl_add_u32 v42, v33, 13, v39\n" \
"v_xor_b32 v39, 8, v38\n" \
"v_lshlrev_b32 v39, 4, v39\n" \
"v_lshl_add_u32 v43, v33, 13, v39\n" \
"v_lshrrev_b32 v39, 5, v32\n" \
"s_and_b32 s62, s63, 1\n" \
"s_lshl_b32 s62, s62, 3\n" \
"v_lshl_or_b32 v45, v39, 2, s62\n" \
"v_and_b32 v46, 31, v32\n" \
"v_xor_b32 v45, v45, v46\n" \
"v_lshlrev_b32 v45, 4, v45\n" \
"v_lshl_add_u32 v44, v39, 10, v45\n" \
"s_cmp_lt_u32 s63, 2\n" \
"s_cselect_b64 s[40:41], %1, %2\n" \
"s_and_b32 s41, s41, 0xffff\n" \
"s_mov_b32 s42, 0x400000\n" \
"s_mov_b32 s43, 0x20000\n" \
"s_and_b32 s62, s63, 1\n" \
"s_lshl_b32 s62, s62, 18\n" \
"s_lshl_b32 s67, s64, 8\n" \
"s_add_u32 s62, s62, s67\n" \
"s_lshl_b32 s67, s63, 13\n" \
"s_add_u32 m0, s67, 0x6000\n" \
"s_nop 0\n" \
"buffer_load_dwordx4 v40, s[40:43], s62 offen lds\n" \
"s_add_u32 s62, s62, 0x8000\n" \
"s_add_u32 m0, m0, 0x400\n" \
"s_nop 0\n" \
"buffer_load_dwordx4 v41, s[40:43], s62 offen lds\n" \
"s_add_u32 s62, s62, 0x8000\n" \
"s_add_u32 m0, m0, 0x400\n" \
"s_nop 0\n" \
"buffer_load_dwordx4 v42, s[40:43], s62 offen lds\n" \
"s_add_u32 s62, s62, 0x8000\n" \
"s_add_u32 m0, m0, 0x400\n" \
"s_nop 0\n" \
"buffer_load_dwordx4 v43, s[40:43], s62 offen lds\n" \
"s_add_u32 s62, s62, 0x8000\n" \
"s_add_u32 m0, m0, 0x400\n" \
"s_nop 0\n" \
"buffer_load_dwordx4 v40, s[40:43], s62 offen lds\n" \
"s_add_u32 s62, s62, 0x8000\n" \
"s_add_u32 m0, m0, 0x400\n" \
"s_nop 0\n" \
"buffer_load_dwordx4 v41, s[40:43], s62 offen lds\n" \
"s_add_u32 s62, s62, 0x8000\n" \
"s_add_u32 m0, m0, 0x400\n" \
"s_nop 0\n" \
"buffer_load_dwordx4 v42, s[40:43], s62 offen lds\n" \
"s_add_u32 s62, s62, 0x8000\n" \
"s_add_u32 m0, m0, 0x400\n" \
"s_nop 0\n" \
"buffer_load_dwordx4 v43, s[40:43], s62 offen lds\n" \
"s_and_b32 s62, s63, 1\n" \
"s_lshl_b32 s62, s62, 18\n" \
"s_lshl_b32 s67, s64, 8\n" \
"s_add_u32 s62, s62, s67\n" \
"s_add_u32 s62, s62, 0x80000\n" \
"s_lshl_b32 s67, s63, 13\n" \
"s_add_u32 m0, s67, 0xe000\n" \
"s_nop 0\n" \
"buffer_load_dwordx4 v40, s[40:43], s62 offen lds\n" \
"s_add_u32 s62, s62, 0x8000\n" \
"s_add_u32 m0, m0, 0x400\n" \
"s_nop 0\n" \
"buffer_load_dwordx4 v41, s[40:43], s62 offen lds\n" \
"s_add_u32 s62, s62, 0x8000\n" \
"s_add_u32 m0, m0, 0x400\n" \
"s_nop 0\n" \
"buffer_load_dwordx4 v42, s[40:43], s62 offen lds\n" \
"s_add_u32 s62, s62, 0x8000\n" \
"s_add_u32 m0, m0, 0x400\n" \
"s_nop 0\n" \
"buffer_load_dwordx4 v43, s[40:43], s62 offen lds\n" \
"s_add_u32 s62, s62, 0x8000\n" \
"s_add_u32 m0, m0, 0x400\n" \
"s_nop 0\n" \
"buffer_load_dwordx4 v40, s[40:43], s62 offen lds\n" \
"s_add_u32 s62, s62, 0x8000\n" \
"s_add_u32 m0, m0, 0x400\n" \
"s_nop 0\n" \
"buffer_load_dwordx4 v41, s[40:43], s62 offen lds\n" \
"s_add_u32 s62, s62, 0x8000\n" \
"s_add_u32 m0, m0, 0x400\n" \
"s_nop 0\n" \
"buffer_load_dwordx4 v42, s[40:43], s62 offen lds\n" \
"s_add_u32 s62, s62, 0x8000\n" \
"s_add_u32 m0, m0, 0x400\n" \
"s_nop 0\n" \
"buffer_load_dwordx4 v43, s[40:43], s62 offen lds\n" \
"s_and_b32 s62, s63, 1\n" \
"s_lshl_b32 s62, s62, 18\n" \
"s_lshl_b32 s67, s64, 8\n" \
"s_add_u32 s62, s62, s67\n" \
"s_add_u32 s62, s62, 0x200000\n" \
"s_lshl_b32 s67, s63, 13\n" \
"s_add_u32 m0, s67, 0x16000\n" \
"s_nop 0\n" \
"buffer_load_dwordx4 v40, s[40:43], s62 offen lds\n" \
"s_add_u32 s62, s62, 0x8000\n" \
"s_add_u32 m0, m0, 0x400\n" \
"s_nop 0\n" \
"buffer_load_dwordx4 v41, s[40:43], s62 offen lds\n" \
"s_add_u32 s62, s62, 0x8000\n" \
"s_add_u32 m0, m0, 0x400\n" \
"s_nop 0\n" \
"buffer_load_dwordx4 v42, s[40:43], s62 offen lds\n" \
"s_add_u32 s62, s62, 0x8000\n" \
"s_add_u32 m0, m0, 0x400\n" \
"s_nop 0\n" \
"buffer_load_dwordx4 v43, s[40:43], s62 offen lds\n" \
"s_add_u32 s62, s62, 0x8000\n" \
"s_add_u32 m0, m0, 0x400\n" \
"s_nop 0\n" \
"buffer_load_dwordx4 v40, s[40:43], s62 offen lds\n" \
"s_add_u32 s62, s62, 0x8000\n" \
"s_add_u32 m0, m0, 0x400\n" \
"s_nop 0\n" \
"buffer_load_dwordx4 v41, s[40:43], s62 offen lds\n" \
"s_add_u32 s62, s62, 0x8000\n" \
"s_add_u32 m0, m0, 0x400\n" \
"s_nop 0\n" \
"buffer_load_dwordx4 v42, s[40:43], s62 offen lds\n" \
"s_add_u32 s62, s62, 0x8000\n" \
"s_add_u32 m0, m0, 0x400\n" \
"s_nop 0\n" \
"buffer_load_dwordx4 v43, s[40:43], s62 offen lds\n" \
"s_and_b32 s62, s63, 1\n" \
"s_lshl_b32 s62, s62, 18\n" \
"s_lshl_b32 s67, s64, 8\n" \
"s_add_u32 s62, s62, s67\n" \
"s_add_u32 s62, s62, 0x280000\n" \
"s_lshl_b32 s67, s63, 13\n" \
"s_add_u32 m0, s67, 0x1e000\n" \
"s_nop 0\n" \
"buffer_load_dwordx4 v40, s[40:43], s62 offen lds\n" \
"s_add_u32 s62, s62, 0x8000\n" \
"s_add_u32 m0, m0, 0x400\n" \
"s_nop 0\n" \
"buffer_load_dwordx4 v41, s[40:43], s62 offen lds\n" \
"s_add_u32 s62, s62, 0x8000\n" \
"s_add_u32 m0, m0, 0x400\n" \
"s_nop 0\n" \
"buffer_load_dwordx4 v42, s[40:43], s62 offen lds\n" \
"s_add_u32 s62, s62, 0x8000\n" \
"s_add_u32 m0, m0, 0x400\n" \
"s_nop 0\n" \
"buffer_load_dwordx4 v43, s[40:43], s62 offen lds\n" \
"s_add_u32 s62, s62, 0x8000\n" \
"s_add_u32 m0, m0, 0x400\n" \
"s_nop 0\n" \
"buffer_load_dwordx4 v40, s[40:43], s62 offen lds\n" \
"s_add_u32 s62, s62, 0x8000\n" \
"s_add_u32 m0, m0, 0x400\n" \
"s_nop 0\n" \
"buffer_load_dwordx4 v41, s[40:43], s62 offen lds\n" \
"s_add_u32 s62, s62, 0x8000\n" \
"s_add_u32 m0, m0, 0x400\n" \
"s_nop 0\n" \
"buffer_load_dwordx4 v42, s[40:43], s62 offen lds\n" \
"s_add_u32 s62, s62, 0x8000\n" \
"s_add_u32 m0, m0, 0x400\n" \
"s_nop 0\n" \
"buffer_load_dwordx4 v43, s[40:43], s62 offen lds\n" \
"v_lshrrev_b32 v39, 1, v33\n" \
"v_and_b32 v46, 1, v35\n" \
"v_lshrrev_b32 v47, 1, v35\n" \
"v_lshrrev_b32 v49, 1, v36\n" \
"v_and_b32 v50, 1, v36\n" \
"v_lshlrev_b32 v50, 3, v50\n" \
"v_lshl_add_u32 v48, v37, 1, v46\n" \
"v_lshl_add_u32 v48, v48, 1, v47\n" \
"v_lshlrev_b32 v48, 1, v48\n" \
"v_lshlrev_b32 v51, 4, v39\n" \
"v_lshl_add_u32 v51, v37, 3, v51\n" \
"v_lshl_add_u32 v51, v46, 1, v51\n" \
"v_add_u32 v51, v51, v47\n" \
"s_lshl_b32 s62, s63, 2\n" \
"v_add_u32 v52, s62, v49\n" \
"v_xor_b32 v52, v52, v48\n" \
"v_lshl_add_u32 v53, v51, 8, v50\n" \
"v_lshl_add_u32 v54, v52, 4, v53\n" \
"v_lshl_add_u32 v55, v47, 14, v54\n" \
"v_xor_b32 v56, 0x120, v55\n" \
"v_lshlrev_b32 v51, 4, v37\n" \
"v_lshl_add_u32 v51, v39, 2, v51\n" \
"v_add_u32 v51, v51, v46\n" \
"v_lshl_add_u32 v48, v37, 1, v46\n" \
"v_lshlrev_b32 v48, 2, v48\n" \
"s_lshl_b32 s62, s63, 2\n" \
"v_add_u32 v52, s62, v49\n" \
"v_lshl_add_u32 v52, v47, 1, v52\n" \
"v_xor_b32 v52, v52, v48\n" \
"v_lshl_add_u32 v53, v51, 9, v50\n" \
"v_lshl_add_u32 v57, v52, 4, v53\n" \
"v_add_u32 v57, 0x6000, v57\n" \
"v_xor_b32 v58, 0x100, v57\n" \
"v_mov_b32 v1, 0xeeee\n" \
"v_mov_b32 v39, 0x4444\n" \
"v_cmp_gt_u32 vcc, 8, v34\n" \
"s_mov_b64 s[54:55], vcc\n" \
"s_nop 0\n" \
"v_cndmask_b32 v1, v1, v39, vcc\n" \
"s_lshl_b32 s62, s63, 10\n" \
"s_add_u32 s62, s62, 0x4000\n" \
"v_lshlrev_b32 v2, 5, v34\n" \
"v_lshl_add_u32 v2, v33, 3, v2\n" \
"v_add_u32 v2, s62, v2\n" \
"v_lshl_add_u32 v39, v33, 2, v35\n" \
"v_lshlrev_b32 v39, 5, v39\n" \
"v_add_u32 v39, 0x4000, v39\n" \
"v_add_u32 v3, v39, v50\n" \
"s_lshl_b32 s62, s63, 10\n" \
"v_add_u32 v4, s62, v3\n" \
"v_lshrrev_b32 v39, 3, v34\n" \
"v_and_b32 v46, 7, v34\n" \
"v_lshlrev_b32 v6, 7, v33\n" \
"v_lshl_add_u32 v6, v46, 4, v6\n" \
"s_lshl_b32 s62, s63, 9\n" \
"v_add_u32 v5, s62, v6\n" \
"v_lshl_add_u32 v5, v39, 11, v5\n" \
"s_lshl_b32 s62, s63, 14\n" \
"s_add_u32 s62, s62, 0x16000\n" \
"v_add_u32 v7, s62, v0\n" \
"s_lshl_b32 s62, s63, 11\n" \
"v_add_u32 v8, s62, v0\n" \
"v_mov_b32 v24, v0\n" \
"v_add_u32 v25, 0xe000, v8\n" \
"v_lshrrev_b32 v47, 1, v33\n" \
"v_lshlrev_b32 v9, 12, v37\n" \
"v_lshl_add_u32 v9, v47, 14, v9\n" \
"v_lshl_add_u32 v9, v34, 3, v9\n" \
"s_lshl_b32 s62, s63, 3\n" \
"s_add_u32 s62, s62, s64\n" \
"v_lshl_add_u32 v47, v33, 1, s62\n" \
"v_and_b32 v47, 31, v47\n" \
"v_lshl_add_u32 v9, v47, 7, v9\n" \
"s_lshl_b32 s62, s66, 7\n" \
"s_cmp_eq_u32 s63, 0\n" \
"s_cselect_b32 s62, s62, 0x7f000000\n" \
"v_mov_b32 v10, 0x7f000000\n" \
"v_writelane_b32 v10, s62, 0\n" \
"v_lshlrev_b32 v11, 10, v46\n" \
"v_lshl_add_u32 v11, v33, 5, v11\n" \
"v_lshl_add_u32 v11, v39, 4, v11\n" \
"v_bfe_u32 v23, v34, 2, 1\n" \
"v_lshlrev_b32 v23, 7, v23\n" \
"v_lshl_add_u32 v23, v33, 8, v23\n" \
"v_lshl_add_u32 v23, v39, 6, v23\n" \
"v_lshl_add_u32 v23, v36, 4, v23\n" \
"v_mov_b32 v16, 0x3f803f80\n" \
"v_mov_b32 v17, 0x3f803f80\n" \
"v_mov_b32 v18, 0x3f803f80\n" \
"v_mov_b32 v19, 0x3f803f80\n" \
"v_cmp_gt_u32 vcc, 2, v33\n" \
"s_nop 1\n" \
"v_cndmask_b32 v20, 0, v16, vcc\n" \
"v_mov_b32 v21, v20\n" \
"s_mov_b32 s58, 0x3e0293ee\n" \
"v_add_u32 v60, 0x6000, v55\n" \
"v_xor_b32 v61, 32, v60\n" \
"v_add_u32 v62, 0x6000, v56\n" \
"v_xor_b32 v63, 32, v62\n" \
"s_waitcnt vmcnt(24)\n" \
"s_barrier\n" \
"ds_read_b64_tr_b16 a[0:1], v60 offset:0\n" \
"ds_read_b64_tr_b16 a[2:3], v60 offset:1024\n" \
"ds_read_b64_tr_b16 a[4:5], v60 offset:8192\n" \
"ds_read_b64_tr_b16 a[6:7], v60 offset:9216\n" \
"ds_read_b64_tr_b16 a[8:9], v61 offset:0\n" \
"ds_read_b64_tr_b16 a[10:11], v61 offset:1024\n" \
"ds_read_b64_tr_b16 a[12:13], v61 offset:8192\n" \
"ds_read_b64_tr_b16 a[14:15], v61 offset:9216\n" \
"ds_read_b64_tr_b16 a[16:17], v62 offset:0\n" \
"ds_read_b64_tr_b16 a[18:19], v62 offset:1024\n" \
"ds_read_b64_tr_b16 a[20:21], v62 offset:8192\n" \
"ds_read_b64_tr_b16 a[22:23], v62 offset:9216\n" \
"ds_read_b64_tr_b16 a[24:25], v63 offset:0\n" \
"ds_read_b64_tr_b16 a[26:27], v63 offset:1024\n" \
"ds_read_b64_tr_b16 a[28:29], v63 offset:8192\n" \
"ds_read_b64_tr_b16 a[30:31], v63 offset:9216\n" \
"s_waitcnt lgkmcnt(0)\n" \
"s_barrier\n" \
"s_and_b32 s62, s63, 1\n" \
"s_lshl_b32 s62, s62, 18\n" \
"s_lshl_b32 s67, s64, 8\n" \
"s_add_u32 s62, s62, s67\n" \
"s_add_u32 s62, s62, 0x100000\n" \
"s_lshl_b32 s67, s63, 13\n" \
"s_add_u32 m0, s67, 0x6000\n" \
"s_nop 0\n" \
"buffer_load_dwordx4 v40, s[40:43], s62 offen lds\n" \
"s_add_u32 s62, s62, 0x8000\n" \
"s_add_u32 m0, m0, 0x400\n" \
"s_nop 0\n" \
"buffer_load_dwordx4 v41, s[40:43], s62 offen lds\n" \
"s_add_u32 s62, s62, 0x8000\n" \
"s_add_u32 m0, m0, 0x400\n" \
"s_nop 0\n" \
"buffer_load_dwordx4 v42, s[40:43], s62 offen lds\n" \
"s_add_u32 s62, s62, 0x8000\n" \
"s_add_u32 m0, m0, 0x400\n" \
"s_nop 0\n" \
"buffer_load_dwordx4 v43, s[40:43], s62 offen lds\n" \
"s_add_u32 s62, s62, 0x8000\n" \
"s_add_u32 m0, m0, 0x400\n" \
"s_nop 0\n" \
"buffer_load_dwordx4 v40, s[40:43], s62 offen lds\n" \
"s_add_u32 s62, s62, 0x8000\n" \
"s_add_u32 m0, m0, 0x400\n" \
"s_nop 0\n" \
"buffer_load_dwordx4 v41, s[40:43], s62 offen lds\n" \
"s_add_u32 s62, s62, 0x8000\n" \
"s_add_u32 m0, m0, 0x400\n" \
"s_nop 0\n" \
"buffer_load_dwordx4 v42, s[40:43], s62 offen lds\n" \
"s_add_u32 s62, s62, 0x8000\n" \
"s_add_u32 m0, m0, 0x400\n" \
"s_nop 0\n" \
"buffer_load_dwordx4 v43, s[40:43], s62 offen lds\n" \
"v_add_u32 v60, 0xe000, v55\n" \
"v_xor_b32 v61, 32, v60\n" \
"v_add_u32 v62, 0xe000, v56\n" \
"v_xor_b32 v63, 32, v62\n" \
"s_waitcnt vmcnt(24)\n" \
"s_barrier\n" \
"ds_read_b64_tr_b16 a[32:33], v60 offset:0\n" \
"ds_read_b64_tr_b16 a[34:35], v60 offset:1024\n" \
"ds_read_b64_tr_b16 a[36:37], v60 offset:8192\n" \
"ds_read_b64_tr_b16 a[38:39], v60 offset:9216\n" \
"ds_read_b64_tr_b16 a[40:41], v61 offset:0\n" \
"ds_read_b64_tr_b16 a[42:43], v61 offset:1024\n" \
"ds_read_b64_tr_b16 a[44:45], v61 offset:8192\n" \
"ds_read_b64_tr_b16 a[46:47], v61 offset:9216\n" \
"ds_read_b64_tr_b16 a[48:49], v62 offset:0\n" \
"ds_read_b64_tr_b16 a[50:51], v62 offset:1024\n" \
"ds_read_b64_tr_b16 a[52:53], v62 offset:8192\n" \
"ds_read_b64_tr_b16 a[54:55], v62 offset:9216\n" \
"ds_read_b64_tr_b16 a[56:57], v63 offset:0\n" \
"ds_read_b64_tr_b16 a[58:59], v63 offset:1024\n" \
"ds_read_b64_tr_b16 a[60:61], v63 offset:8192\n" \
"ds_read_b64_tr_b16 a[62:63], v63 offset:9216\n" \
"s_waitcnt lgkmcnt(0)\n" \
"s_barrier\n" \
"s_and_b32 s62, s63, 1\n" \
"s_lshl_b32 s62, s62, 18\n" \
"s_lshl_b32 s67, s64, 8\n" \
"s_add_u32 s62, s62, s67\n" \
"s_add_u32 s62, s62, 0x180000\n" \
"s_lshl_b32 s67, s63, 13\n" \
"s_add_u32 m0, s67, 0xe000\n" \
"s_nop 0\n" \
"buffer_load_dwordx4 v40, s[40:43], s62 offen lds\n" \
"s_add_u32 s62, s62, 0x8000\n" \
"s_add_u32 m0, m0, 0x400\n" \
"s_nop 0\n" \
"buffer_load_dwordx4 v41, s[40:43], s62 offen lds\n" \
"s_add_u32 s62, s62, 0x8000\n" \
"s_add_u32 m0, m0, 0x400\n" \
"s_nop 0\n" \
"buffer_load_dwordx4 v42, s[40:43], s62 offen lds\n" \
"s_add_u32 s62, s62, 0x8000\n" \
"s_add_u32 m0, m0, 0x400\n" \
"s_nop 0\n" \
"buffer_load_dwordx4 v43, s[40:43], s62 offen lds\n" \
"s_add_u32 s62, s62, 0x8000\n" \
"s_add_u32 m0, m0, 0x400\n" \
"s_nop 0\n" \
"buffer_load_dwordx4 v40, s[40:43], s62 offen lds\n" \
"s_add_u32 s62, s62, 0x8000\n" \
"s_add_u32 m0, m0, 0x400\n" \
"s_nop 0\n" \
"buffer_load_dwordx4 v41, s[40:43], s62 offen lds\n" \
"s_add_u32 s62, s62, 0x8000\n" \
"s_add_u32 m0, m0, 0x400\n" \
"s_nop 0\n" \
"buffer_load_dwordx4 v42, s[40:43], s62 offen lds\n" \
"s_add_u32 s62, s62, 0x8000\n" \
"s_add_u32 m0, m0, 0x400\n" \
"s_nop 0\n" \
"buffer_load_dwordx4 v43, s[40:43], s62 offen lds\n" \
"v_add_u32 v60, 0x16000, v55\n" \
"v_xor_b32 v61, 32, v60\n" \
"v_add_u32 v62, 0x16000, v56\n" \
"v_xor_b32 v63, 32, v62\n" \
"s_waitcnt vmcnt(24)\n" \
"s_barrier\n" \
"ds_read_b64_tr_b16 a[64:65], v60 offset:0\n" \
"ds_read_b64_tr_b16 a[66:67], v60 offset:1024\n" \
"ds_read_b64_tr_b16 a[68:69], v60 offset:8192\n" \
"ds_read_b64_tr_b16 a[70:71], v60 offset:9216\n" \
"ds_read_b64_tr_b16 a[72:73], v61 offset:0\n" \
"ds_read_b64_tr_b16 a[74:75], v61 offset:1024\n" \
"ds_read_b64_tr_b16 a[76:77], v61 offset:8192\n" \
"ds_read_b64_tr_b16 a[78:79], v61 offset:9216\n" \
"ds_read_b64_tr_b16 a[80:81], v62 offset:0\n" \
"ds_read_b64_tr_b16 a[82:83], v62 offset:1024\n" \
"ds_read_b64_tr_b16 a[84:85], v62 offset:8192\n" \
"ds_read_b64_tr_b16 a[86:87], v62 offset:9216\n" \
"ds_read_b64_tr_b16 a[88:89], v63 offset:0\n" \
"ds_read_b64_tr_b16 a[90:91], v63 offset:1024\n" \
"ds_read_b64_tr_b16 a[92:93], v63 offset:8192\n" \
"ds_read_b64_tr_b16 a[94:95], v63 offset:9216\n" \
"s_waitcnt lgkmcnt(0)\n" \
"s_barrier\n" \
"s_and_b32 s62, s63, 1\n" \
"s_lshl_b32 s62, s62, 18\n" \
"s_lshl_b32 s67, s64, 8\n" \
"s_add_u32 s62, s62, s67\n" \
"s_add_u32 s62, s62, 0x300000\n" \
"s_lshl_b32 s67, s63, 13\n" \
"s_add_u32 m0, s67, 0x16000\n" \
"s_nop 0\n" \
"buffer_load_dwordx4 v40, s[40:43], s62 offen lds\n" \
"s_add_u32 s62, s62, 0x8000\n" \
"s_add_u32 m0, m0, 0x400\n" \
"s_nop 0\n" \
"buffer_load_dwordx4 v41, s[40:43], s62 offen lds\n" \
"s_add_u32 s62, s62, 0x8000\n" \
"s_add_u32 m0, m0, 0x400\n" \
"s_nop 0\n" \
"buffer_load_dwordx4 v42, s[40:43], s62 offen lds\n" \
"s_add_u32 s62, s62, 0x8000\n" \
"s_add_u32 m0, m0, 0x400\n" \
"s_nop 0\n" \
"buffer_load_dwordx4 v43, s[40:43], s62 offen lds\n" \
"s_add_u32 s62, s62, 0x8000\n" \
"s_add_u32 m0, m0, 0x400\n" \
"s_nop 0\n" \
"buffer_load_dwordx4 v40, s[40:43], s62 offen lds\n" \
"s_add_u32 s62, s62, 0x8000\n" \
"s_add_u32 m0, m0, 0x400\n" \
"s_nop 0\n" \
"buffer_load_dwordx4 v41, s[40:43], s62 offen lds\n" \
"s_add_u32 s62, s62, 0x8000\n" \
"s_add_u32 m0, m0, 0x400\n" \
"s_nop 0\n" \
"buffer_load_dwordx4 v42, s[40:43], s62 offen lds\n" \
"s_add_u32 s62, s62, 0x8000\n" \
"s_add_u32 m0, m0, 0x400\n" \
"s_nop 0\n" \
"buffer_load_dwordx4 v43, s[40:43], s62 offen lds\n" \
"v_add_u32 v60, 0x1e000, v55\n" \
"v_xor_b32 v61, 32, v60\n" \
"v_add_u32 v62, 0x1e000, v56\n" \
"v_xor_b32 v63, 32, v62\n" \
"s_waitcnt vmcnt(24)\n" \
"s_barrier\n" \
"ds_read_b64_tr_b16 a[96:97], v60 offset:0\n" \
"ds_read_b64_tr_b16 a[98:99], v60 offset:1024\n" \
"ds_read_b64_tr_b16 a[100:101], v60 offset:8192\n" \
"ds_read_b64_tr_b16 a[102:103], v60 offset:9216\n" \
"ds_read_b64_tr_b16 a[104:105], v61 offset:0\n" \
"ds_read_b64_tr_b16 a[106:107], v61 offset:1024\n" \
"ds_read_b64_tr_b16 a[108:109], v61 offset:8192\n" \
"ds_read_b64_tr_b16 a[110:111], v61 offset:9216\n" \
"ds_read_b64_tr_b16 a[112:113], v62 offset:0\n" \
"ds_read_b64_tr_b16 a[114:115], v62 offset:1024\n" \
"ds_read_b64_tr_b16 a[116:117], v62 offset:8192\n" \
"ds_read_b64_tr_b16 a[118:119], v62 offset:9216\n" \
"ds_read_b64_tr_b16 a[120:121], v63 offset:0\n" \
"ds_read_b64_tr_b16 a[122:123], v63 offset:1024\n" \
"ds_read_b64_tr_b16 a[124:125], v63 offset:8192\n" \
"ds_read_b64_tr_b16 a[126:127], v63 offset:9216\n" \
"s_waitcnt lgkmcnt(0)\n" \
"s_barrier\n" \
"s_and_b32 s62, s63, 1\n" \
"s_lshl_b32 s62, s62, 18\n" \
"s_lshl_b32 s67, s64, 8\n" \
"s_add_u32 s62, s62, s67\n" \
"s_add_u32 s62, s62, 0x380000\n" \
"s_lshl_b32 s67, s63, 13\n" \
"s_add_u32 m0, s67, 0x1e000\n" \
"s_nop 0\n" \
"buffer_load_dwordx4 v40, s[40:43], s62 offen lds\n" \
"s_add_u32 s62, s62, 0x8000\n" \
"s_add_u32 m0, m0, 0x400\n" \
"s_nop 0\n" \
"buffer_load_dwordx4 v41, s[40:43], s62 offen lds\n" \
"s_add_u32 s62, s62, 0x8000\n" \
"s_add_u32 m0, m0, 0x400\n" \
"s_nop 0\n" \
"buffer_load_dwordx4 v42, s[40:43], s62 offen lds\n" \
"s_add_u32 s62, s62, 0x8000\n" \
"s_add_u32 m0, m0, 0x400\n" \
"s_nop 0\n" \
"buffer_load_dwordx4 v43, s[40:43], s62 offen lds\n" \
"s_add_u32 s62, s62, 0x8000\n" \
"s_add_u32 m0, m0, 0x400\n" \
"s_nop 0\n" \
"buffer_load_dwordx4 v40, s[40:43], s62 offen lds\n" \
"s_add_u32 s62, s62, 0x8000\n" \
"s_add_u32 m0, m0, 0x400\n" \
"s_nop 0\n" \
"buffer_load_dwordx4 v41, s[40:43], s62 offen lds\n" \
"s_add_u32 s62, s62, 0x8000\n" \
"s_add_u32 m0, m0, 0x400\n" \
"s_nop 0\n" \
"buffer_load_dwordx4 v42, s[40:43], s62 offen lds\n" \
"s_add_u32 s62, s62, 0x8000\n" \
"s_add_u32 m0, m0, 0x400\n" \
"s_nop 0\n" \
"buffer_load_dwordx4 v43, s[40:43], s62 offen lds\n" \
"v_add_u32 v60, 0x6000, v55\n" \
"v_xor_b32 v61, 32, v60\n" \
"v_add_u32 v62, 0x6000, v56\n" \
"v_xor_b32 v63, 32, v62\n" \
"s_waitcnt vmcnt(24)\n" \
"s_barrier\n" \
"ds_read_b64_tr_b16 a[128:129], v60 offset:0\n" \
"ds_read_b64_tr_b16 a[130:131], v60 offset:1024\n" \
"ds_read_b64_tr_b16 a[132:133], v60 offset:8192\n" \
"ds_read_b64_tr_b16 a[134:135], v60 offset:9216\n" \
"ds_read_b64_tr_b16 a[136:137], v61 offset:0\n" \
"ds_read_b64_tr_b16 a[138:139], v61 offset:1024\n" \
"ds_read_b64_tr_b16 a[140:141], v61 offset:8192\n" \
"ds_read_b64_tr_b16 a[142:143], v61 offset:9216\n" \
"ds_read_b64_tr_b16 a[144:145], v62 offset:0\n" \
"ds_read_b64_tr_b16 a[146:147], v62 offset:1024\n" \
"ds_read_b64_tr_b16 a[148:149], v62 offset:8192\n" \
"ds_read_b64_tr_b16 a[150:151], v62 offset:9216\n" \
"ds_read_b64_tr_b16 a[152:153], v63 offset:0\n" \
"ds_read_b64_tr_b16 a[154:155], v63 offset:1024\n" \
"ds_read_b64_tr_b16 a[156:157], v63 offset:8192\n" \
"ds_read_b64_tr_b16 a[158:159], v63 offset:9216\n" \
"s_waitcnt lgkmcnt(0)\n" \
"s_barrier\n" \
"s_mov_b64 s[40:41], %3\n" \
"s_and_b32 s41, s41, 0xffff\n" \
"s_mov_b32 s42, 0x400000\n" \
"s_mov_b32 s43, 0x20000\n" \
"s_lshl_b32 s62, s63, 18\n" \
"s_lshl_b32 s67, s64, 8\n" \
"s_add_u32 s62, s62, s67\n" \
"s_lshl_b32 s67, s63, 13\n" \
"s_add_u32 m0, s67, 0x6000\n" \
"s_nop 0\n" \
"buffer_load_dwordx4 v40, s[40:43], s62 offen lds\n" \
"s_add_u32 s62, s62, 0x8000\n" \
"s_add_u32 m0, m0, 0x400\n" \
"s_nop 0\n" \
"buffer_load_dwordx4 v41, s[40:43], s62 offen lds\n" \
"s_add_u32 s62, s62, 0x8000\n" \
"s_add_u32 m0, m0, 0x400\n" \
"s_nop 0\n" \
"buffer_load_dwordx4 v42, s[40:43], s62 offen lds\n" \
"s_add_u32 s62, s62, 0x8000\n" \
"s_add_u32 m0, m0, 0x400\n" \
"s_nop 0\n" \
"buffer_load_dwordx4 v43, s[40:43], s62 offen lds\n" \
"s_add_u32 s62, s62, 0x8000\n" \
"s_add_u32 m0, m0, 0x400\n" \
"s_nop 0\n" \
"buffer_load_dwordx4 v40, s[40:43], s62 offen lds\n" \
"s_add_u32 s62, s62, 0x8000\n" \
"s_add_u32 m0, m0, 0x400\n" \
"s_nop 0\n" \
"buffer_load_dwordx4 v41, s[40:43], s62 offen lds\n" \
"s_add_u32 s62, s62, 0x8000\n" \
"s_add_u32 m0, m0, 0x400\n" \
"s_nop 0\n" \
"buffer_load_dwordx4 v42, s[40:43], s62 offen lds\n" \
"s_add_u32 s62, s62, 0x8000\n" \
"s_add_u32 m0, m0, 0x400\n" \
"s_nop 0\n" \
"buffer_load_dwordx4 v43, s[40:43], s62 offen lds\n" \
"v_add_u32 v60, 0xe000, v55\n" \
"v_xor_b32 v61, 32, v60\n" \
"v_add_u32 v62, 0xe000, v56\n" \
"v_xor_b32 v63, 32, v62\n" \
"s_waitcnt vmcnt(24)\n" \
"s_barrier\n" \
"ds_read_b64_tr_b16 a[160:161], v60 offset:0\n" \
"ds_read_b64_tr_b16 a[162:163], v60 offset:1024\n" \
"ds_read_b64_tr_b16 a[164:165], v60 offset:8192\n" \
"ds_read_b64_tr_b16 a[166:167], v60 offset:9216\n" \
"ds_read_b64_tr_b16 a[168:169], v61 offset:0\n" \
"ds_read_b64_tr_b16 a[170:171], v61 offset:1024\n" \
"ds_read_b64_tr_b16 a[172:173], v61 offset:8192\n" \
"ds_read_b64_tr_b16 a[174:175], v61 offset:9216\n" \
"ds_read_b64_tr_b16 a[176:177], v62 offset:0\n" \
"ds_read_b64_tr_b16 a[178:179], v62 offset:1024\n" \
"ds_read_b64_tr_b16 a[180:181], v62 offset:8192\n" \
"ds_read_b64_tr_b16 a[182:183], v62 offset:9216\n" \
"ds_read_b64_tr_b16 a[184:185], v63 offset:0\n" \
"ds_read_b64_tr_b16 a[186:187], v63 offset:1024\n" \
"ds_read_b64_tr_b16 a[188:189], v63 offset:8192\n" \
"ds_read_b64_tr_b16 a[190:191], v63 offset:9216\n" \
"s_waitcnt lgkmcnt(0)\n" \
"s_barrier\n" \
"s_lshl_b32 s62, s63, 18\n" \
"s_lshl_b32 s67, s64, 8\n" \
"s_add_u32 s62, s62, s67\n" \
"s_add_u32 s62, s62, 0x200000\n" \
"s_lshl_b32 s67, s63, 13\n" \
"s_add_u32 m0, s67, 0xe000\n" \
"s_nop 0\n" \
"buffer_load_dwordx4 v40, s[40:43], s62 offen lds\n" \
"s_add_u32 s62, s62, 0x8000\n" \
"s_add_u32 m0, m0, 0x400\n" \
"s_nop 0\n" \
"buffer_load_dwordx4 v41, s[40:43], s62 offen lds\n" \
"s_add_u32 s62, s62, 0x8000\n" \
"s_add_u32 m0, m0, 0x400\n" \
"s_nop 0\n" \
"buffer_load_dwordx4 v42, s[40:43], s62 offen lds\n" \
"s_add_u32 s62, s62, 0x8000\n" \
"s_add_u32 m0, m0, 0x400\n" \
"s_nop 0\n" \
"buffer_load_dwordx4 v43, s[40:43], s62 offen lds\n" \
"s_add_u32 s62, s62, 0x8000\n" \
"s_add_u32 m0, m0, 0x400\n" \
"s_nop 0\n" \
"buffer_load_dwordx4 v40, s[40:43], s62 offen lds\n" \
"s_add_u32 s62, s62, 0x8000\n" \
"s_add_u32 m0, m0, 0x400\n" \
"s_nop 0\n" \
"buffer_load_dwordx4 v41, s[40:43], s62 offen lds\n" \
"s_add_u32 s62, s62, 0x8000\n" \
"s_add_u32 m0, m0, 0x400\n" \
"s_nop 0\n" \
"buffer_load_dwordx4 v42, s[40:43], s62 offen lds\n" \
"s_add_u32 s62, s62, 0x8000\n" \
"s_add_u32 m0, m0, 0x400\n" \
"s_nop 0\n" \
"buffer_load_dwordx4 v43, s[40:43], s62 offen lds\n" \
"v_add_u32 v60, 0x16000, v55\n" \
"v_xor_b32 v61, 32, v60\n" \
"v_add_u32 v62, 0x16000, v56\n" \
"v_xor_b32 v63, 32, v62\n" \
"s_waitcnt vmcnt(24)\n" \
"s_barrier\n" \
"ds_read_b64_tr_b16 a[192:193], v60 offset:0\n" \
"ds_read_b64_tr_b16 a[194:195], v60 offset:1024\n" \
"ds_read_b64_tr_b16 a[196:197], v60 offset:8192\n" \
"ds_read_b64_tr_b16 a[198:199], v60 offset:9216\n" \
"ds_read_b64_tr_b16 a[200:201], v61 offset:0\n" \
"ds_read_b64_tr_b16 a[202:203], v61 offset:1024\n" \
"ds_read_b64_tr_b16 a[204:205], v61 offset:8192\n" \
"ds_read_b64_tr_b16 a[206:207], v61 offset:9216\n" \
"ds_read_b64_tr_b16 a[208:209], v62 offset:0\n" \
"ds_read_b64_tr_b16 a[210:211], v62 offset:1024\n" \
"ds_read_b64_tr_b16 a[212:213], v62 offset:8192\n" \
"ds_read_b64_tr_b16 a[214:215], v62 offset:9216\n" \
"ds_read_b64_tr_b16 a[216:217], v63 offset:0\n" \
"ds_read_b64_tr_b16 a[218:219], v63 offset:1024\n" \
"ds_read_b64_tr_b16 a[220:221], v63 offset:8192\n" \
"ds_read_b64_tr_b16 a[222:223], v63 offset:9216\n" \
"s_waitcnt lgkmcnt(0)\n" \
"s_barrier\n" \
"s_lshl_b32 s62, s63, 18\n" \
"s_lshl_b32 s67, s64, 8\n" \
"s_add_u32 s62, s62, s67\n" \
"s_add_u32 s62, s62, 0x100000\n" \
"s_lshl_b32 s67, s63, 13\n" \
"s_add_u32 m0, s67, 0x16000\n" \
"s_nop 0\n" \
"buffer_load_dwordx4 v40, s[40:43], s62 offen lds\n" \
"s_add_u32 s62, s62, 0x8000\n" \
"s_add_u32 m0, m0, 0x400\n" \
"s_nop 0\n" \
"buffer_load_dwordx4 v41, s[40:43], s62 offen lds\n" \
"s_add_u32 s62, s62, 0x8000\n" \
"s_add_u32 m0, m0, 0x400\n" \
"s_nop 0\n" \
"buffer_load_dwordx4 v42, s[40:43], s62 offen lds\n" \
"s_add_u32 s62, s62, 0x8000\n" \
"s_add_u32 m0, m0, 0x400\n" \
"s_nop 0\n" \
"buffer_load_dwordx4 v43, s[40:43], s62 offen lds\n" \
"s_add_u32 s62, s62, 0x8000\n" \
"s_add_u32 m0, m0, 0x400\n" \
"s_nop 0\n" \
"buffer_load_dwordx4 v40, s[40:43], s62 offen lds\n" \
"s_add_u32 s62, s62, 0x8000\n" \
"s_add_u32 m0, m0, 0x400\n" \
"s_nop 0\n" \
"buffer_load_dwordx4 v41, s[40:43], s62 offen lds\n" \
"s_add_u32 s62, s62, 0x8000\n" \
"s_add_u32 m0, m0, 0x400\n" \
"s_nop 0\n" \
"buffer_load_dwordx4 v42, s[40:43], s62 offen lds\n" \
"s_add_u32 s62, s62, 0x8000\n" \
"s_add_u32 m0, m0, 0x400\n" \
"s_nop 0\n" \
"buffer_load_dwordx4 v43, s[40:43], s62 offen lds\n" \
"v_add_u32 v60, 0x1e000, v55\n" \
"v_xor_b32 v61, 32, v60\n" \
"v_add_u32 v62, 0x1e000, v56\n" \
"v_xor_b32 v63, 32, v62\n" \
"s_waitcnt vmcnt(24)\n" \
"s_barrier\n" \
"ds_read_b64_tr_b16 a[224:225], v60 offset:0\n" \
"ds_read_b64_tr_b16 a[226:227], v60 offset:1024\n" \
"ds_read_b64_tr_b16 a[228:229], v60 offset:8192\n" \
"ds_read_b64_tr_b16 a[230:231], v60 offset:9216\n" \
"ds_read_b64_tr_b16 a[232:233], v61 offset:0\n" \
"ds_read_b64_tr_b16 a[234:235], v61 offset:1024\n" \
"ds_read_b64_tr_b16 a[236:237], v61 offset:8192\n" \
"ds_read_b64_tr_b16 a[238:239], v61 offset:9216\n" \
"ds_read_b64_tr_b16 a[240:241], v62 offset:0\n" \
"ds_read_b64_tr_b16 a[242:243], v62 offset:1024\n" \
"ds_read_b64_tr_b16 a[244:245], v62 offset:8192\n" \
"ds_read_b64_tr_b16 a[246:247], v62 offset:9216\n" \
"ds_read_b64_tr_b16 a[248:249], v63 offset:0\n" \
"ds_read_b64_tr_b16 a[250:251], v63 offset:1024\n" \
"ds_read_b64_tr_b16 a[252:253], v63 offset:8192\n" \
"ds_read_b64_tr_b16 a[254:255], v63 offset:9216\n" \
"s_waitcnt lgkmcnt(0)\n" \
"s_barrier\n" \
"s_lshl_b32 s62, s63, 18\n" \
"s_lshl_b32 s67, s64, 8\n" \
"s_add_u32 s62, s62, s67\n" \
"s_add_u32 s62, s62, 0x300000\n" \
"s_lshl_b32 s67, s63, 13\n" \
"s_add_u32 m0, s67, 0x1e000\n" \
"s_nop 0\n" \
"buffer_load_dwordx4 v40, s[40:43], s62 offen lds\n" \
"s_add_u32 s62, s62, 0x8000\n" \
"s_add_u32 m0, m0, 0x400\n" \
"s_nop 0\n" \
"buffer_load_dwordx4 v41, s[40:43], s62 offen lds\n" \
"s_add_u32 s62, s62, 0x8000\n" \
"s_add_u32 m0, m0, 0x400\n" \
"s_nop 0\n" \
"buffer_load_dwordx4 v42, s[40:43], s62 offen lds\n" \
"s_add_u32 s62, s62, 0x8000\n" \
"s_add_u32 m0, m0, 0x400\n" \
"s_nop 0\n" \
"buffer_load_dwordx4 v43, s[40:43], s62 offen lds\n" \
"s_add_u32 s62, s62, 0x8000\n" \
"s_add_u32 m0, m0, 0x400\n" \
"s_nop 0\n" \
"buffer_load_dwordx4 v40, s[40:43], s62 offen lds\n" \
"s_add_u32 s62, s62, 0x8000\n" \
"s_add_u32 m0, m0, 0x400\n" \
"s_nop 0\n" \
"buffer_load_dwordx4 v41, s[40:43], s62 offen lds\n" \
"s_add_u32 s62, s62, 0x8000\n" \
"s_add_u32 m0, m0, 0x400\n" \
"s_nop 0\n" \
"buffer_load_dwordx4 v42, s[40:43], s62 offen lds\n" \
"s_add_u32 s62, s62, 0x8000\n" \
"s_add_u32 m0, m0, 0x400\n" \
"s_nop 0\n" \
"buffer_load_dwordx4 v43, s[40:43], s62 offen lds\n" \
"v_add_u32 v60, 0x6000, v54\n" \
"v_xor_b32 v61, 32, v60\n" \
"s_waitcnt vmcnt(24)\n" \
"s_barrier\n" \
"ds_read_b64_tr_b16 v[128:129], v60 offset:0\n" \
"ds_read_b64_tr_b16 v[130:131], v60 offset:1024\n" \
"ds_read_b64_tr_b16 v[132:133], v60 offset:8192\n" \
"ds_read_b64_tr_b16 v[134:135], v60 offset:9216\n" \
"ds_read_b64_tr_b16 v[136:137], v61 offset:0\n" \
"ds_read_b64_tr_b16 v[138:139], v61 offset:1024\n" \
"ds_read_b64_tr_b16 v[140:141], v61 offset:8192\n" \
"ds_read_b64_tr_b16 v[142:143], v61 offset:9216\n" \
"ds_read_b64_tr_b16 v[144:145], v60 offset:16384\n" \
"ds_read_b64_tr_b16 v[146:147], v60 offset:17408\n" \
"ds_read_b64_tr_b16 v[148:149], v60 offset:24576\n" \
"ds_read_b64_tr_b16 v[150:151], v60 offset:25600\n" \
"ds_read_b64_tr_b16 v[152:153], v61 offset:16384\n" \
"ds_read_b64_tr_b16 v[154:155], v61 offset:17408\n" \
"ds_read_b64_tr_b16 v[156:157], v61 offset:24576\n" \
"ds_read_b64_tr_b16 v[158:159], v61 offset:25600\n" \
"s_waitcnt lgkmcnt(0)\n" \
"s_barrier\n" \
"s_mov_b64 s[40:41], %4\n" \
"s_and_b32 s41, s41, 0xffff\n" \
"s_mov_b32 s42, 0x400000\n" \
"s_mov_b32 s43, 0x20000\n" \
"s_lshl_b32 s62, s64, 17\n" \
"s_lshl_b32 s67, s63, 14\n" \
"s_add_u32 s62, s62, s67\n" \
"s_lshl_b32 s67, s65, 9\n" \
"s_add_u32 s62, s62, s67\n" \
"s_lshl_b32 s67, s63, 13\n" \
"s_add_u32 m0, s67, 0x6000\n" \
"s_nop 0\n" \
"buffer_load_dwordx4 v44, s[40:43], s62 offen lds\n" \
"s_add_u32 s62, s62, 0x800\n" \
"s_add_u32 m0, m0, 0x400\n" \
"s_nop 0\n" \
"buffer_load_dwordx4 v44, s[40:43], s62 offen lds\n" \
"s_add_u32 s62, s62, 0x800\n" \
"s_add_u32 m0, m0, 0x400\n" \
"s_nop 0\n" \
"buffer_load_dwordx4 v44, s[40:43], s62 offen lds\n" \
"s_add_u32 s62, s62, 0x800\n" \
"s_add_u32 m0, m0, 0x400\n" \
"s_nop 0\n" \
"buffer_load_dwordx4 v44, s[40:43], s62 offen lds\n" \
"s_add_u32 s62, s62, 0x800\n" \
"s_add_u32 m0, m0, 0x400\n" \
"s_nop 0\n" \
"buffer_load_dwordx4 v44, s[40:43], s62 offen lds\n" \
"s_add_u32 s62, s62, 0x800\n" \
"s_add_u32 m0, m0, 0x400\n" \
"s_nop 0\n" \
"buffer_load_dwordx4 v44, s[40:43], s62 offen lds\n" \
"s_add_u32 s62, s62, 0x800\n" \
"s_add_u32 m0, m0, 0x400\n" \
"s_nop 0\n" \
"buffer_load_dwordx4 v44, s[40:43], s62 offen lds\n" \
"s_add_u32 s62, s62, 0x800\n" \
"s_add_u32 m0, m0, 0x400\n" \
"s_nop 0\n" \
"buffer_load_dwordx4 v44, s[40:43], s62 offen lds\n" \
"v_add_u32 v60, 0xe000, v54\n" \
"v_xor_b32 v61, 32, v60\n" \
"s_waitcnt vmcnt(24)\n" \
"s_barrier\n" \
"ds_read_b64_tr_b16 v[160:161], v60 offset:0\n" \
"ds_read_b64_tr_b16 v[162:163], v60 offset:1024\n" \
"ds_read_b64_tr_b16 v[164:165], v60 offset:8192\n" \
"ds_read_b64_tr_b16 v[166:167], v60 offset:9216\n" \
"ds_read_b64_tr_b16 v[168:169], v61 offset:0\n" \
"ds_read_b64_tr_b16 v[170:171], v61 offset:1024\n" \
"ds_read_b64_tr_b16 v[172:173], v61 offset:8192\n" \
"ds_read_b64_tr_b16 v[174:175], v61 offset:9216\n" \
"ds_read_b64_tr_b16 v[176:177], v60 offset:16384\n" \
"ds_read_b64_tr_b16 v[178:179], v60 offset:17408\n" \
"ds_read_b64_tr_b16 v[180:181], v60 offset:24576\n" \
"ds_read_b64_tr_b16 v[182:183], v60 offset:25600\n" \
"ds_read_b64_tr_b16 v[184:185], v61 offset:16384\n" \
"ds_read_b64_tr_b16 v[186:187], v61 offset:17408\n" \
"ds_read_b64_tr_b16 v[188:189], v61 offset:24576\n" \
"ds_read_b64_tr_b16 v[190:191], v61 offset:25600\n" \
"s_waitcnt lgkmcnt(0)\n" \
"s_barrier\n" \
"v_and_b32 v22, 0x70, v0\n" \
"v_lshlrev_b32 v22, 3, v22\n" \
"s_mov_b32 s67, 0x400000\n" \
"buffer_load_dwordx2 v[32:33], v22, s[36:39], s67 offen sc1\n" \
"s_lshl_b32 s62, s64, 17\n" \
"s_lshl_b32 s67, s63, 14\n" \
"s_add_u32 s62, s62, s67\n" \
"s_lshl_b32 s67, s65, 9\n" \
"s_add_u32 s62, s62, s67\n" \
"s_add_u32 s62, s62, 0x10000\n" \
"s_lshl_b32 s67, s63, 13\n" \
"s_add_u32 m0, s67, 0xe000\n" \
"s_nop 0\n" \
"buffer_load_dwordx4 v44, s[40:43], s62 offen lds\n" \
"s_add_u32 s62, s62, 0x800\n" \
"s_add_u32 m0, m0, 0x400\n" \
"s_nop 0\n" \
"buffer_load_dwordx4 v44, s[40:43], s62 offen lds\n" \
"s_add_u32 s62, s62, 0x800\n" \
"s_add_u32 m0, m0, 0x400\n" \
"s_nop 0\n" \
"buffer_load_dwordx4 v44, s[40:43], s62 offen lds\n" \
"s_add_u32 s62, s62, 0x800\n" \
"s_add_u32 m0, m0, 0x400\n" \
"s_nop 0\n" \
"buffer_load_dwordx4 v44, s[40:43], s62 offen lds\n" \
"s_add_u32 s62, s62, 0x800\n" \
"s_add_u32 m0, m0, 0x400\n" \
"s_nop 0\n" \
"buffer_load_dwordx4 v44, s[40:43], s62 offen lds\n" \
"s_add_u32 s62, s62, 0x800\n" \
"s_add_u32 m0, m0, 0x400\n" \
"s_nop 0\n" \
"buffer_load_dwordx4 v44, s[40:43], s62 offen lds\n" \
"s_add_u32 s62, s62, 0x800\n" \
"s_add_u32 m0, m0, 0x400\n" \
"s_nop 0\n" \
"buffer_load_dwordx4 v44, s[40:43], s62 offen lds\n" \
"s_add_u32 s62, s62, 0x800\n" \
"s_add_u32 m0, m0, 0x400\n" \
"s_nop 0\n" \
"buffer_load_dwordx4 v44, s[40:43], s62 offen lds\n" \
"v_add_u32 v60, 0x16000, v54\n" \
"v_xor_b32 v61, 32, v60\n" \
"s_waitcnt vmcnt(24)\n" \
"s_barrier\n" \
"ds_read_b64_tr_b16 v[192:193], v60 offset:0\n" \
"ds_read_b64_tr_b16 v[194:195], v60 offset:1024\n" \
"ds_read_b64_tr_b16 v[196:197], v60 offset:8192\n" \
"ds_read_b64_tr_b16 v[198:199], v60 offset:9216\n" \
"ds_read_b64_tr_b16 v[200:201], v61 offset:0\n" \
"ds_read_b64_tr_b16 v[202:203], v61 offset:1024\n" \
"ds_read_b64_tr_b16 v[204:205], v61 offset:8192\n" \
"ds_read_b64_tr_b16 v[206:207], v61 offset:9216\n" \
"ds_read_b64_tr_b16 v[208:209], v60 offset:16384\n" \
"ds_read_b64_tr_b16 v[210:211], v60 offset:17408\n" \
"ds_read_b64_tr_b16 v[212:213], v60 offset:24576\n" \
"ds_read_b64_tr_b16 v[214:215], v60 offset:25600\n" \
"ds_read_b64_tr_b16 v[216:217], v61 offset:16384\n" \
"ds_read_b64_tr_b16 v[218:219], v61 offset:17408\n" \
"ds_read_b64_tr_b16 v[220:221], v61 offset:24576\n" \
"ds_read_b64_tr_b16 v[222:223], v61 offset:25600\n" \
"s_waitcnt lgkmcnt(0)\n" \
"s_barrier\n" \
"v_add_u32 v60, 0x1e000, v54\n" \
"v_xor_b32 v61, 32, v60\n" \
"s_waitcnt vmcnt(16)\n" \
"s_barrier\n" \
"ds_read_b64_tr_b16 v[224:225], v60 offset:0\n" \
"ds_read_b64_tr_b16 v[226:227], v60 offset:1024\n" \
"ds_read_b64_tr_b16 v[228:229], v60 offset:8192\n" \
"ds_read_b64_tr_b16 v[230:231], v60 offset:9216\n" \
"ds_read_b64_tr_b16 v[232:233], v61 offset:0\n" \
"ds_read_b64_tr_b16 v[234:235], v61 offset:1024\n" \
"ds_read_b64_tr_b16 v[236:237], v61 offset:8192\n" \
"ds_read_b64_tr_b16 v[238:239], v61 offset:9216\n" \
"ds_read_b64_tr_b16 v[240:241], v60 offset:16384\n" \
"ds_read_b64_tr_b16 v[242:243], v60 offset:17408\n" \
"ds_read_b64_tr_b16 v[244:245], v60 offset:24576\n" \
"ds_read_b64_tr_b16 v[246:247], v60 offset:25600\n" \
"ds_read_b64_tr_b16 v[248:249], v61 offset:16384\n" \
"ds_read_b64_tr_b16 v[250:251], v61 offset:17408\n" \
"ds_read_b64_tr_b16 v[252:253], v61 offset:24576\n" \
"ds_read_b64_tr_b16 v[254:255], v61 offset:25600\n" \
"s_waitcnt lgkmcnt(0)\n" \
"s_barrier\n" \
"s_waitcnt vmcnt(8)\n" \
"s_barrier\n" \
"ds_read_b64_tr_b16 v[72:73], v57 offset:0\n" \
"ds_read_b64_tr_b16 v[74:75], v57 offset:1024\n" \
"ds_read_b64_tr_b16 v[76:77], v57 offset:4096\n" \
"ds_read_b64_tr_b16 v[78:79], v57 offset:5120\n" \
"ds_read_b64_tr_b16 v[80:81], v58 offset:0\n" \
"ds_read_b64_tr_b16 v[82:83], v58 offset:1024\n" \
"ds_read_b64_tr_b16 v[84:85], v58 offset:4096\n" \
"ds_read_b64_tr_b16 v[86:87], v58 offset:5120\n" \
"ds_read_b64_tr_b16 v[88:89], v57 offset:16384\n" \
"ds_read_b64_tr_b16 v[90:91], v57 offset:17408\n" \
"ds_read_b64_tr_b16 v[92:93], v57 offset:20480\n" \
"ds_read_b64_tr_b16 v[94:95], v57 offset:21504\n" \
"ds_read_b64_tr_b16 v[96:97], v58 offset:16384\n" \
"ds_read_b64_tr_b16 v[98:99], v58 offset:17408\n" \
"ds_read_b64_tr_b16 v[100:101], v58 offset:20480\n" \
"ds_read_b64_tr_b16 v[102:103], v58 offset:21504\n" \
"s_waitcnt lgkmcnt(0)\n" \
"ds_write_b128 v7, v[72:75] offset:0\n" \
"ds_write_b128 v7, v[76:79] offset:1024\n" \
"ds_write_b128 v7, v[80:83] offset:8192\n" \
"ds_write_b128 v7, v[84:87] offset:9216\n" \
"ds_write_b128 v7, v[88:91] offset:2048\n" \
"ds_write_b128 v7, v[92:95] offset:3072\n" \
"ds_write_b128 v7, v[96:99] offset:10240\n" \
"ds_write_b128 v7, v[100:103] offset:11264\n" \
"s_waitcnt lgkmcnt(0)\n" \
"s_waitcnt vmcnt(0)\n" \
"s_barrier\n" \
"ds_read_b64_tr_b16 v[72:73], v57 offset:32768\n" \
"ds_read_b64_tr_b16 v[74:75], v57 offset:33792\n" \
"ds_read_b64_tr_b16 v[76:77], v57 offset:36864\n" \
"ds_read_b64_tr_b16 v[78:79], v57 offset:37888\n" \
"ds_read_b64_tr_b16 v[80:81], v58 offset:32768\n" \
"ds_read_b64_tr_b16 v[82:83], v58 offset:33792\n" \
"ds_read_b64_tr_b16 v[84:85], v58 offset:36864\n" \
"ds_read_b64_tr_b16 v[86:87], v58 offset:37888\n" \
"ds_read_b64_tr_b16 v[88:89], v57 offset:49152\n" \
"ds_read_b64_tr_b16 v[90:91], v57 offset:50176\n" \
"ds_read_b64_tr_b16 v[92:93], v57 offset:53248\n" \
"ds_read_b64_tr_b16 v[94:95], v57 offset:54272\n" \
"ds_read_b64_tr_b16 v[96:97], v58 offset:49152\n" \
"ds_read_b64_tr_b16 v[98:99], v58 offset:50176\n" \
"ds_read_b64_tr_b16 v[100:101], v58 offset:53248\n" \
"ds_read_b64_tr_b16 v[102:103], v58 offset:54272\n" \
"s_waitcnt lgkmcnt(0)\n" \
"ds_write_b128 v7, v[72:75] offset:4096\n" \
"ds_write_b128 v7, v[76:79] offset:5120\n" \
"ds_write_b128 v7, v[80:83] offset:12288\n" \
"ds_write_b128 v7, v[84:87] offset:13312\n" \
"ds_write_b128 v7, v[88:91] offset:6144\n" \
"ds_write_b128 v7, v[92:95] offset:7168\n" \
"ds_write_b128 v7, v[96:99] offset:14336\n" \
"ds_write_b128 v7, v[100:103] offset:15360\n" \
"s_waitcnt lgkmcnt(0)\n" \
"s_mov_b32 s59, 0\n" \
"s_mov_b32 s67, 0x400000\n" \
".Lch_boot_%=:\n" \
"v_and_b32 v34, 0xffff0000, v32\n" \
"v_cmp_ne_u32 vcc, s4, v34\n" \
"v_cmp_ne_u32 s[78:79], s5, v33\n" \
"s_or_b64 vcc, vcc, s[78:79]\n" \
"s_cbranch_vccz .Lch_bootok_%=\n" \
"buffer_load_dwordx2 v[32:33], v22, s[36:39], s67 offen sc1\n" \
"s_waitcnt vmcnt(0)\n" \
"s_add_u32 s59, s59, 1\n" \
"s_cmp_lt_u32 s59, 0x100000\n" \
"s_cbranch_scc1 .Lch_boot_%=\n" \
".Lch_bootok_%=:\n" \
"v_readlane_b32 s62, v32, 0\n" \
"v_readlane_b32 s67, v32, 1\n" \
"s_sub_i32 s78, s62, s67\n" \
"v_readlane_b32 s62, v32, 3\n" \
"v_readlane_b32 s67, v32, 2\n" \
"s_sub_i32 s79, s62, s67\n" \
"s_abs_i32 s62, s78\n" \
"s_abs_i32 s67, s79\n" \
"s_min_i32 s62, s62, s67\n" \
"s_add_i32 s67, s78, s79\n" \
"s_cmp_lt_i32 s62, 150\n" \
"s_cselect_b32 s78, s67, s78\n" \
"s_cselect_b32 s79, s67, s79\n" \
"s_cmp_gt_i32 s78, 0\n" \
"s_cselect_b32 s95, 1, 0\n" \
"s_cmp_gt_i32 s79, 0\n" \
"s_cselect_b32 s62, 0, 2\n" \
"s_or_b32 s95, s95, s62\n" \
"s_mov_b32 s44, 0\n" \
"s_lshl_b32 s92, s65, 17\n" \
"s_lshl_b32 s62, s63, 15\n" \
"s_add_u32 s92, s92, s62\n" \
"s_lshr_b32 s62, s95, 1\n" \
"s_lshl_b32 s62, s62, 13\n" \
"s_add_u32 s92, s92, s62\n" \
"s_add_u32 s92, s92, 0x200000\n" \
"s_mov_b32 s94, 0\n" \
"s_and_b32 s62, s95, 1\n" \
"s_lshl_b32 s45, s62, 13\n" \
"s_add_u32 s46, s45, 0x1000\n" \
"s_add_u32 s49, s45, 0x100000\n" \
"s_and_b32 s94, s94, 0x1fffff\n" \
"s_add_u32 s47, s94, s92\n" \
"s_andn2_b32 s67, 1, s95\n" \
"s_lshl_b32 s67, s67, 13\n" \
"s_lshl_b32 s62, s66, 7\n" \
"s_add_u32 s62, s62, s67\n" \
"s_add_u32 s62, s62, 0x444000\n" \
"s_xor_b32 s62, s62, 0x2000\n" \
"s_cmp_eq_u32 s63, 0\n" \
"s_cselect_b32 s62, s62, 0x7f000000\n" \
"v_mov_b32 v13, 0x7f000000\n" \
"v_writelane_b32 v13, s62, 0\n" \
"s_lshl_b32 s62, s64, 7\n" \
"s_add_u32 s62, s62, s67\n" \
"s_add_u32 s67, s62, 0x449000\n" \
"v_mov_b32 v14, s67\n" \
"s_add_u32 s62, s62, 0x440400\n" \
"v_writelane_b32 v14, s62, 0\n" \
"s_lshr_b32 s62, s64, 1\n" \
"s_lshl_b32 s62, s62, 7\n" \
"s_and_b32 s67, s67, 0x2000\n" \
"s_add_u32 s62, s62, s67\n" \
"s_add_u32 s62, s62, 0x448000\n" \
"v_writelane_b32 v14, s62, 2\n" \
"s_add_u32 s62, s62, 0x800\n" \
"v_writelane_b32 v14, s62, 3\n" \
"v_mov_b32 v26, 0\n" \
"v_mov_b32 v15, v10\n" \
"s_lshl_b32 s50, s64, 14\n" \
"s_add_u32 s50, s50, 0x2000000\n" \
"s_bitset1_b32 s50, 13\n" \
"s_bfe_u32 s62, s50, 0x10015\n" \
"s_lshr_b32 s62, s95, s62\n" \
"s_and_b32 s62, s62, 1\n" \
"s_lshl_b32 s62, s62, 13\n" \
"s_xor_b32 s50, s50, s62\n" \
"s_add_u32 s51, s50, 0x2000000\n" \
"s_mov_b64 s[30:31], 0\n" \
"s_mov_b64 s[56:57], -1\n" \
"s_mov_b32 s68, 0\n" \
"s_mov_b32 s69, 0\n" \
"s_mov_b32 s80, 0\n" \
"s_mov_b32 s81, 0\n" \
"s_mov_b32 s82, 0\n" \
"s_mov_b32 s83, 0\n" \
"s_mov_b32 s84, 0\n" \
"s_mov_b32 s85, 0\n" \
"s_mov_b32 s86, 0\n" \
"s_mov_b32 s87, 0\n" \
"s_mov_b32 s88, 0\n" \
"s_mov_b32 s89, 0\n" \
"s_mov_b32 s90, 0\n" \
"s_lshl_b32 s90, s63, 11\n" \
"s_add_u32 s99, s90, 0xe000\n" \
"s_mov_b32 s93, s99\n" \
"s_mov_b32 s60, 0\n" \
"s_barrier\n" \
"s_mov_b32 s59, 0\n" \
".Lch_kvc0_%=:\n" \
"v_readlane_b32 s91, v26, 2\n" \
"v_readlane_b32 s62, v26, 3\n" \
"s_min_u32 s91, s91, s62\n" \
"s_add_u32 s91, s91, 18\n" \
"v_readlane_b32 s62, v26, 0\n" \
"s_cmp_eq_u32 s62, 64\n" \
"s_cselect_b32 s62, 0x100, s62\n" \
"s_min_u32 s91, s91, s62\n" \
"v_readlane_b32 s62, v26, 1\n" \
"s_cmp_eq_u32 s62, 64\n" \
"s_cselect_b32 s62, 0x100, s62\n" \
"s_min_u32 s91, s91, s62\n" \
"s_cmp_ge_u32 s91, 1\n" \
"s_cbranch_scc1 .Lch_kvok0_%=\n" \
"s_sleep 1\n" \
"buffer_load_dwordx2 v[26:27], v14, s[36:39], 0 offen sc1\n" \
"s_waitcnt vmcnt(0)\n" \
"s_add_u32 s59, s59, 1\n" \
"s_cmp_lt_u32 s59, 0x40000\n" \
"s_cbranch_scc1 .Lch_kvc0_%=\n" \
".Lch_kvok0_%=:\n" \
"s_mov_b32 s59, 0\n" \
"s_mov_b32 m0, s90\n" \
"s_nop 0\n" \
"buffer_load_dwordx4 v8, s[36:39], s50 offen sc1 lds\n" \
"buffer_load_dwordx4 v8, s[36:39], s50 offen offset:1024 sc1 lds\n" \
"s_mov_b32 m0, s93\n" \
"s_nop 0\n" \
"buffer_load_dwordx4 v8, s[36:39], s51 offen sc1 lds\n" \
"buffer_load_dwordx4 v8, s[36:39], s51 offen offset:1024 sc1 lds\n" \
"s_and_b32 s52, s48, 0x7fffff\n" \
"s_add_u32 s52, s52, 0x8000000\n" \
"s_xor_b32 s52, s52, 0x2000\n" \
"s_mov_b32 s48, s50\n" \
"s_lshl_b32 s67, s63, 10\n" \
"s_add_u32 s53, s52, s67\n" \
"s_add_u32 s53, s53, 0x1000\n" \
"s_add_u32 s50, s50, 0x80000\n" \
"s_bitset1_b32 s50, 13\n" \
"s_bfe_u32 s67, s50, 0x10015\n" \
"s_lshr_b32 s67, s95, s67\n" \
"s_and_b32 s67, s67, 1\n" \
"s_lshl_b32 s67, s67, 13\n" \
"s_xor_b32 s50, s50, s67\n" \
"s_add_u32 s51, s50, 0x2000000\n" \
"s_xor_b32 s90, s90, 0x8000\n" \
"s_add_u32 s60, s60, 0x2000\n" \
"s_and_b32 s60, s60, 0x7fff\n" \
"s_add_u32 s93, s99, s60\n" \
"s_and_b32 s52, s48, 0x7fffff\n" \
"s_add_u32 s52, s52, 0x8000000\n" \
"s_xor_b32 s52, s52, 0x2000\n" \
"s_mov_b32 s48, s50\n" \
"s_lshl_b32 s67, s63, 10\n" \
"s_add_u32 s53, s52, s67\n" \
"s_add_u32 s53, s53, 0x1000\n" \
"s_add_u32 s50, s50, 0x80000\n" \
"s_bitset1_b32 s50, 13\n" \
"s_bfe_u32 s67, s50, 0x10015\n" \
"s_lshr_b32 s67, s95, s67\n" \
"s_and_b32 s67, s67, 1\n" \
"s_lshl_b32 s67, s67, 13\n" \
"s_xor_b32 s50, s50, s67\n" \
"s_add_u32 s51, s50, 0x2000000\n" \
"s_xor_b32 s90, s90, 0x8000\n" \
"s_add_u32 s60, s60, 0x2000\n" \
"s_and_b32 s60, s60, 0x7fff\n" \
"s_add_u32 s93, s99, s60\n" \
"s_memtime s[42:43]\n" \
"s_memrealtime s[80:81]\n" \
"s_nop 2\n" \
"s_memrealtime s[82:83]\n" \
"s_nop 2\n" \
"s_memrealtime s[84:85]\n" \
"s_nop 2\n" \
"s_memrealtime s[86:87]\n" \
"s_nop 2\n" \
"s_memrealtime s[88:89]\n" \
"s_nop 2\n" \
"s_memrealtime s[96:97]\n" \
"s_nop 2\n" \
"s_memrealtime s[40:41]\n" \
"s_mov_b32 s62, 0\n" \
"s_waitcnt lgkmcnt(0)\n" \
"s_cmp_eq_u32 s82, s80\n" \
"s_addc_u32 s62, s62, 0\n" \
"s_cmp_eq_u32 s84, s80\n" \
"s_addc_u32 s62, s62, 0\n" \
"s_cmp_eq_u32 s86, s80\n" \
"s_addc_u32 s62, s62, 0\n" \
"s_cmp_eq_u32 s88, s80\n" \
"s_addc_u32 s62, s62, 0\n" \
"s_cmp_eq_u32 s96, s80\n" \
"s_addc_u32 s62, s62, 0\n" \
"s_cmp_eq_u32 s40, s80\n" \
"s_addc_u32 s62, s62, 0\n" \
"s_lshl4_add_u32 s42, s62, s42\n" \
"s_mul_i32 s43, s80, 382\n" \
"s_lshl_b32 s42, s42, 4\n" \
"s_sub_u32 s29, s43, s42\n" \
"s_mov_b32 s28, 33600\n" \
"s_add_u32 s98, s29, s28\n" \
"s_waitcnt vmcnt(0)\n" \
"s_barrier\n" \
"s_cmp_eq_u32 s63, 1\n" \
"s_cbranch_scc1 .Lch_sl1_%=\n" \
"v_lshlrev_b32 v14, 3, v0\n" \
"s_cmp_eq_u32 s63, 2\n" \
"s_cselect_b32 s62, s49, 0x7f000000\n" \
"v_add_u32 v14, s62, v14\n" \
".Lch_sl1_%=:\n" \
"ds_read_b128 v[64:67], v24 offset:0\n" \
"ds_read_b128 v[68:71], v24 offset:4096\n" \
"ds_read_b128 v[72:75], v24 offset:1024\n" \
"ds_read_b128 v[76:79], v24 offset:5120\n" \
"ds_read_b128 v[80:83], v24 offset:2048\n" \
"ds_read_b128 v[84:87], v24 offset:6144\n" \
"ds_read_b128 v[88:91], v24 offset:3072\n" \
"ds_read_b128 v[92:95], v24 offset:7168\n" \
"s_memrealtime s[74:75]\n" \
"s_memtime s[72:73]\n" \
"s_waitcnt lgkmcnt(0)\n" \
"s_sub_u32 s71, s74, s70\n" \
"s_mov_b32 s70, s74\n" \
"s_memrealtime s[78:79]\n" \
"s_waitcnt lgkmcnt(0)\n" \
"s_mul_i32 s75, s78, 382\n" \
"s_sub_u32 s75, s75, 0x40000000\n" \
"s_mov_b32 s34, 46400\n" \
"s_mov_b32 s35, 0\n" \
"s_mov_b32 s27, 0\n" \
"s_mov_b32 s33, 2\n" \
"s_lshl_b32 s62, s66, 2\n" \
"s_add_u32 s62, s62, s63\n" \
"s_cmp_eq_u32 s62, 2\n" \
"s_cselect_b32 s62, 64, 0\n" \
"v_lshlrev_b32 v12, 3, v0\n" \
"v_add_u32 v12, 4, v12\n" \
"v_mov_b32 v22, 0x7f000000\n" \
"v_cmp_gt_u32 vcc, s62, v0\n" \
"s_nop 1\n" \
"v_cndmask_b32 v12, v22, v12, vcc\n" \
"s_and_b32 s75, s75, -2\n" \
"s_lshl_b32 s62, s27, 16\n" \
"s_or_b32 s62, s62, s35\n" \
"v_mov_b32 v22, s75\n" \
"v_writelane_b32 v22, s34, 1\n" \
"v_writelane_b32 v22, s62, 2\n" \
"v_writelane_b32 v22, s33, 3\n" \
"buffer_store_dword v22, v12, s[36:39], s49 offen sc1\n" \
"s_mov_b32 s59, 0\n" \
"s_waitcnt lgkmcnt(0)\n" \
"s_branch .Lch_loop_%=\n" \
"s_nop 0\n" \
".Lch_loopb_%=:\n" \
"s_barrier\n" \
".Lch_loop_%=:\n" \
"buffer_load_dwordx4 v[32:35], v23, s[36:39], s45 offen offset:0 sc1\n" \
"v_mov_b64 v[96:97], 0\n" \
"s_nop 2\n" \
"v_mov_b32 v22, v22\n" \
"s_nop 2\n" \
"v_mov_b32 v22, v22\n" \
"s_nop 2\n" \
"v_mov_b32 v22, v22\n" \
"s_nop 1\n" \
"buffer_load_dwordx4 v[36:39], v23, s[36:39], s45 offen offset:1024 sc1\n" \
"v_mov_b64 v[98:99], 0\n" \
"s_nop 2\n" \
"v_mov_b32 v22, v22\n" \
"s_nop 2\n" \
"v_mov_b32 v22, v22\n" \
"s_nop 2\n" \
"v_mov_b32 v22, v22\n" \
"s_nop 1\n" \
"buffer_load_dwordx4 v[40:43], v23, s[36:39], s45 offen offset:2048 sc1\n" \
"v_mov_b64 v[100:101], 0\n" \
"s_nop 2\n" \
"v_mov_b32 v22, v22\n" \
"s_nop 2\n" \
"v_mov_b32 v22, v22\n" \
"s_nop 2\n" \
"v_mov_b32 v22, v22\n" \
"s_nop 1\n" \
"buffer_load_dwordx4 v[44:47], v23, s[36:39], s45 offen offset:3072 sc1\n" \
"v_mov_b64 v[102:103], 0\n" \
"s_nop 2\n" \
"v_mov_b32 v22, v22\n" \
"s_nop 2\n" \
"v_mov_b32 v22, v22\n" \
"s_nop 2\n" \
"v_mov_b32 v22, v22\n" \
"s_nop 1\n" \
"buffer_load_dwordx4 v[48:51], v23, s[36:39], s46 offen offset:0 sc1\n" \
"v_mov_b64 v[104:105], 0\n" \
"s_nop 2\n" \
"v_mov_b32 v22, v22\n" \
"s_nop 2\n" \
"v_mov_b32 v22, v22\n" \
"s_nop 2\n" \
"v_mov_b32 v22, v22\n" \
"s_nop 1\n" \
"buffer_load_dwordx4 v[52:55], v23, s[36:39], s46 offen offset:1024 sc1\n" \
"v_mov_b64 v[106:107], 0\n" \
"v_mov_b64 v[108:109], 0\n" \
"v_mov_b64 v[110:111], 0\n" \
".Lch_body_%=:\n" \
"s_nop 2\n" \
"v_mov_b32 v22, v22\n" \
"s_nop 2\n" \
"v_mov_b32 v22, v22\n" \
"s_nop 1\n" \
"s_waitcnt vmcnt(5)\n" \
"s_memtime s[72:73]\n" \
"v_smfmac_f32_16x16x64_bf16 v[96:99], v[32:35], a[0:7], v1\n" \
"buffer_load_dwordx4 v[56:59], v23, s[36:39], s46 offen offset:2048 sc1\n" \
"v_mov_b32_dpp v112, v32 row_ror:8 row_mask:0xf bank_mask:0xf\n" \
"v_smfmac_f32_16x16x64_bf16 v[100:103], v[32:35], a[8:15], v1\n" \
"v_mov_b32_dpp v113, v33 row_ror:8 row_mask:0xf bank_mask:0xf\n" \
"v_smfmac_f32_16x16x64_bf16 v[104:107], v[32:35], v[128:135], v1\n" \
"v_mov_b32_dpp v114, v34 row_ror:8 row_mask:0xf bank_mask:0xf\n" \
"v_smfmac_f32_16x16x64_bf16 v[108:111], v[32:35], v[136:143], v1\n" \
"v_mov_b32_dpp v115, v35 row_ror:8 row_mask:0xf bank_mask:0xf\n" \
"s_waitcnt vmcnt(5)\n" \
"v_smfmac_f32_16x16x64_bf16 v[96:99], v[36:39], a[32:39], v1\n" \
"buffer_load_dwordx4 v[60:63], v23, s[36:39], s46 offen offset:3072 sc1\n" \
"v_mov_b32_dpp v116, v36 row_ror:8 row_mask:0xf bank_mask:0xf\n" \
"s_waitcnt lgkmcnt(0)\n" \
"v_smfmac_f32_16x16x64_bf16 v[100:103], v[36:39], a[40:47], v1\n" \
"v_mov_b32_dpp v117, v37 row_ror:8 row_mask:0xf bank_mask:0xf\n" \
"s_lshl4_add_u32 s73, s72, s98\n" \
"v_smfmac_f32_16x16x64_bf16 v[96:99], v[112:115], a[16:23], v1\n" \
"v_mov_b32_dpp v118, v38 row_ror:8 row_mask:0xf bank_mask:0xf\n" \
"v_smfmac_f32_16x16x64_bf16 v[100:103], v[112:115], a[24:31], v1\n" \
"v_mov_b32_dpp v119, v39 row_ror:8 row_mask:0xf bank_mask:0xf\n" \
"v_smfmac_f32_16x16x64_bf16 v[104:107], v[36:39], v[144:151], v1\n" \
"s_waitcnt vmcnt(5)\n" \
"v_smfmac_f32_16x16x64_bf16 v[96:99], v[40:43], a[64:71], v1\n" \
"v_mov_b32_dpp v112, v40 row_ror:8 row_mask:0xf bank_mask:0xf\n" \
"v_mov_b32 v11, s73\n" \
"v_smfmac_f32_16x16x64_bf16 v[100:103], v[40:43], a[72:79], v1\n" \
"v_mov_b32_dpp v113, v41 row_ror:8 row_mask:0xf bank_mask:0xf\n" \
"buffer_store_dword v11, v15, s[36:39], s49 offen sc1\n" \
"v_smfmac_f32_16x16x64_bf16 v[96:99], v[116:119], a[48:55], v1\n" \
"v_mov_b32_dpp v114, v42 row_ror:8 row_mask:0xf bank_mask:0xf\n" \
"v_smfmac_f32_16x16x64_bf16 v[100:103], v[116:119], a[56:63], v1\n" \
"v_mov_b32_dpp v115, v43 row_ror:8 row_mask:0xf bank_mask:0xf\n" \
"v_smfmac_f32_16x16x64_bf16 v[108:111], v[36:39], v[152:159], v1\n" \
"s_waitcnt vmcnt(5)\n" \
"v_smfmac_f32_16x16x64_bf16 v[96:99], v[44:47], a[96:103], v1\n" \
"v_mov_b32_dpp v116, v44 row_ror:8 row_mask:0xf bank_mask:0xf\n" \
"v_smfmac_f32_16x16x64_bf16 v[100:103], v[44:47], a[104:111], v1\n" \
"v_mov_b32_dpp v117, v45 row_ror:8 row_mask:0xf bank_mask:0xf\n" \
"v_smfmac_f32_16x16x64_bf16 v[96:99], v[112:115], a[80:87], v1\n" \
"v_mov_b32_dpp v118, v46 row_ror:8 row_mask:0xf bank_mask:0xf\n" \
"v_smfmac_f32_16x16x64_bf16 v[100:103], v[112:115], a[88:95], v1\n" \
"v_mov_b32_dpp v119, v47 row_ror:8 row_mask:0xf bank_mask:0xf\n" \
"v_smfmac_f32_16x16x64_bf16 v[104:107], v[40:43], v[160:167], v1\n" \
"s_waitcnt vmcnt(4)\n" \
"v_smfmac_f32_16x16x64_bf16 v[96:99], v[48:51], a[128:135], v1\n" \
"v_mov_b32_dpp v112, v48 row_ror:8 row_mask:0xf bank_mask:0xf\n" \
"v_smfmac_f32_16x16x64_bf16 v[100:103], v[48:51], a[136:143], v1\n" \
"v_mov_b32_dpp v113, v49 row_ror:8 row_mask:0xf bank_mask:0xf\n" \
"v_smfmac_f32_16x16x64_bf16 v[96:99], v[116:119], a[112:119], v1\n" \
"v_mov_b32_dpp v114, v50 row_ror:8 row_mask:0xf bank_mask:0xf\n" \
"v_smfmac_f32_16x16x64_bf16 v[100:103], v[116:119], a[120:127], v1\n" \
"v_mov_b32_dpp v115, v51 row_ror:8 row_mask:0xf bank_mask:0xf\n" \
"v_smfmac_f32_16x16x64_bf16 v[108:111], v[40:43], v[168:175], v1\n" \
"s_waitcnt vmcnt(3)\n" \
"v_smfmac_f32_16x16x64_bf16 v[96:99], v[52:55], a[160:167], v1\n" \
"v_mov_b32_dpp v116, v52 row_ror:8 row_mask:0xf bank_mask:0xf\n" \
"v_smfmac_f32_16x16x64_bf16 v[100:103], v[52:55], a[168:175], v1\n" \
"v_mov_b32_dpp v117, v53 row_ror:8 row_mask:0xf bank_mask:0xf\n" \
"v_smfmac_f32_16x16x64_bf16 v[96:99], v[112:115], a[144:151], v1\n" \
"v_mov_b32_dpp v118, v54 row_ror:8 row_mask:0xf bank_mask:0xf\n" \
"v_smfmac_f32_16x16x64_bf16 v[100:103], v[112:115], a[152:159], v1\n" \
"v_mov_b32_dpp v119, v55 row_ror:8 row_mask:0xf bank_mask:0xf\n" \
"v_smfmac_f32_16x16x64_bf16 v[104:107], v[44:47], v[176:183], v1\n" \
"s_waitcnt vmcnt(2)\n" \
"v_smfmac_f32_16x16x64_bf16 v[96:99], v[56:59], a[192:199], v1\n" \
"v_mov_b32_dpp v112, v56 row_ror:8 row_mask:0xf bank_mask:0xf\n" \
"v_smfmac_f32_16x16x64_bf16 v[100:103], v[56:59], a[200:207], v1\n" \
"v_mov_b32_dpp v113, v57 row_ror:8 row_mask:0xf bank_mask:0xf\n" \
"v_smfmac_f32_16x16x64_bf16 v[96:99], v[116:119], a[176:183], v1\n" \
"v_mov_b32_dpp v114, v58 row_ror:8 row_mask:0xf bank_mask:0xf\n" \
"v_smfmac_f32_16x16x64_bf16 v[100:103], v[116:119], a[184:191], v1\n" \
"v_mov_b32_dpp v115, v59 row_ror:8 row_mask:0xf bank_mask:0xf\n" \
"s_waitcnt vmcnt(1)\n" \
"v_smfmac_f32_16x16x64_bf16 v[96:99], v[60:63], a[224:231], v1\n" \
"v_mov_b32_dpp v116, v60 row_ror:8 row_mask:0xf bank_mask:0xf\n" \
"v_smfmac_f32_16x16x64_bf16 v[96:99], v[112:115], a[208:215], v1\n" \
"v_mov_b32_dpp v117, v61 row_ror:8 row_mask:0xf bank_mask:0xf\n" \
"v_smfmac_f32_16x16x64_bf16 v[100:103], v[60:63], a[232:239], v1\n" \
"v_mov_b32_dpp v118, v62 row_ror:8 row_mask:0xf bank_mask:0xf\n" \
"v_mov_b32_dpp v119, v63 row_ror:8 row_mask:0xf bank_mask:0xf\n" \
"v_smfmac_f32_16x16x64_bf16 v[100:103], v[112:115], a[216:223], v1\n" \
"s_nop 0\n" \
"v_smfmac_f32_16x16x64_bf16 v[96:99], v[116:119], a[240:247], v1\n" \
"v_smfmac_f32_16x16x64_bf16 v[100:103], v[116:119], a[248:255], v1\n" \
"v_smfmac_f32_16x16x64_bf16 v[108:111], v[44:47], v[184:191], v1\n" \
"v_cvt_pk_bf16_f32 v120, v96, v97\n" \
"v_cvt_pk_bf16_f32 v121, v98, v99\n" \
"ds_write_b64 v2, v[120:121]\n" \
"s_nop 0\n" \
"v_cvt_pk_bf16_f32 v122, v100, v101\n" \
"v_cvt_pk_bf16_f32 v123, v102, v103\n" \
"ds_write_b64 v2, v[122:123] offset:512\n" \
"v_cmp_u_f32 vcc, v96, v97\n" \
"v_cmp_u_f32 s[78:79], v98, v99\n" \
"s_or_b64 vcc, vcc, s[30:31]\n" \
"s_or_b64 vcc, vcc, s[78:79]\n" \
"v_smfmac_f32_16x16x64_bf16 v[104:107], v[48:51], v[192:199], v1\n" \
"s_cbranch_vccnz .Lch_slowA_%=\n" \
".Lch_okA_%=:\n" \
"s_mov_b32 m0, s90\n" \
"v_mov_b32 v11, s44\n" \
"v_smfmac_f32_16x16x64_bf16 v[108:111], v[48:51], v[200:207], v1\n" \
"v_smfmac_f32_16x16x64_bf16 v[104:107], v[52:55], v[208:215], v1\n" \
"s_waitcnt lgkmcnt(0)\n" \
"s_barrier\n" \
"ds_read_b64_tr_b16 v[96:97], v3 offset:0\n" \
"ds_read_b64_tr_b16 v[98:99], v3 offset:512\n" \
"v_smfmac_f32_16x16x64_bf16 v[108:111], v[52:55], v[216:223], v1\n" \
"ds_read_b64_tr_b16 v[112:113], v3 offset:1024\n" \
"ds_read_b64_tr_b16 v[114:115], v3 offset:1536\n" \
"v_smfmac_f32_16x16x64_bf16 v[104:107], v[56:59], v[224:231], v1\n" \
"ds_read_b64_tr_b16 v[120:121], v3 offset:2048\n" \
"ds_read_b64_tr_b16 v[122:123], v3 offset:2560\n" \
"v_smfmac_f32_16x16x64_bf16 v[108:111], v[56:59], v[232:239], v1\n" \
"buffer_store_dword v11, v13, s[36:39], 0 offen sc1\n" \
"ds_read_b64_tr_b16 v[32:33], v3 offset:3072\n" \
"ds_read_b64_tr_b16 v[34:35], v3 offset:3584\n" \
"buffer_load_dwordx2 v[26:27], v14, s[36:39], 0 offen sc1\n" \
"s_waitcnt lgkmcnt(6)\n" \
"v_mfma_f32_16x16x32_bf16 v[40:43], v[64:67], v[96:99], 0\n" \
"ds_read_b64_tr_b16 v[100:101], v3 offset:16\n" \
"ds_read_b64_tr_b16 v[102:103], v3 offset:528\n" \
"v_mfma_f32_16x16x32_bf16 v[44:47], v[68:71], v[96:99], 0\n" \
"ds_read_b64_tr_b16 v[116:117], v3 offset:1040\n" \
"buffer_load_dwordx4 v8, s[36:39], s50 offen sc1 lds\n" \
"s_waitcnt lgkmcnt(7)\n" \
"v_mfma_f32_16x16x32_bf16 v[40:43], v[72:75], v[112:115], v[40:43]\n" \
"ds_read_b64_tr_b16 v[118:119], v3 offset:1552\n" \
"ds_read_b64_tr_b16 v[124:125], v3 offset:2064\n" \
"v_mfma_f32_16x16x32_bf16 v[44:47], v[76:79], v[112:115], v[44:47]\n" \
"ds_read_b64_tr_b16 v[126:127], v3 offset:2576\n" \
"s_waitcnt lgkmcnt(6)\n" \
"v_mfma_f32_16x16x32_bf16 v[40:43], v[80:83], v[120:123], v[40:43]\n" \
"ds_read_b64_tr_b16 v[36:37], v3 offset:3088\n" \
"ds_read_b64_tr_b16 v[38:39], v3 offset:3600\n" \
"v_mfma_f32_16x16x32_bf16 v[44:47], v[84:87], v[120:123], v[44:47]\n" \
"ds_read_b128 v[56:59], v25\n" \
"buffer_load_dwordx4 v8, s[36:39], s50 offen offset:1024 sc1 lds\n" \
"v_mfma_f32_16x16x32_bf16 v[40:43], v[88:91], v[32:35], v[40:43]\n" \
"ds_read_b128 v[48:51], v7 offset:0\n" \
"s_mov_b32 m0, s93\n" \
"v_mfma_f32_16x16x32_bf16 v[44:47], v[92:95], v[32:35], v[44:47]\n" \
"ds_read_b128 v[52:55], v7 offset:1024\n" \
"s_waitcnt lgkmcnt(3)\n" \
"v_mfma_f32_16x16x32_bf16 v[28:31], v[100:103], v[96:99], 0\n" \
"ds_read_b128 v[64:67], v7 offset:2048\n" \
"buffer_load_dwordx4 v8, s[36:39], s51 offen sc1 lds\n" \
"v_mfma_f32_16x16x32_bf16 v[28:31], v[116:119], v[112:115], v[28:31]\n" \
"ds_read_b128 v[68:71], v7 offset:3072\n" \
"v_max3_f32 v22, v40, v41, v42\n" \
"v_mfma_f32_16x16x32_bf16 v[28:31], v[124:127], v[120:123], v[28:31]\n" \
"ds_read_b128 v[72:75], v7 offset:4096\n" \
"v_max3_f32 v22, v22, v43, v44\n" \
"v_mfma_f32_16x16x32_bf16 v[28:31], v[36:39], v[32:35], v[28:31]\n" \
"ds_read_b128 v[76:79], v7 offset:5120\n" \
"buffer_load_dwordx4 v8, s[36:39], s51 offen offset:1024 sc1 lds\n" \
"v_max3_f32 v22, v22, v45, v46\n" \
"v_smfmac_f32_16x16x64_bf16 v[104:107], v[60:63], v[240:247], v1\n" \
"ds_read_b128 v[80:83], v7 offset:6144\n" \
"v_max_f32 v11, v22, v47\n" \
"v_max_f32 v22, v22, v47\n" \
"v_smfmac_f32_16x16x64_bf16 v[108:111], v[60:63], v[248:255], v1\n" \
"ds_read_b128 v[84:87], v7 offset:7168\n" \
"v_permlane32_swap_b32 v22, v11\n" \
"v_cndmask_b32 v40, v44, v40, s[54:55]\n" \
"v_max3_f32 v11, v22, v11, v28\n" \
"v_max3_f32 v11, v11, v29, v30\n" \
"v_max_f32 v22, v11, v31\n" \
"v_max_f32 v11, v11, v31\n" \
"v_cndmask_b32 v41, v45, v41, s[54:55]\n" \
"v_cndmask_b32 v42, v46, v42, s[54:55]\n" \
"v_permlane16_swap_b32 v22, v11\n" \
"v_cndmask_b32 v43, v47, v43, s[54:55]\n" \
"v_max_f32 v11, v22, v11\n" \
"v_mul_f32 v11, 0xbe0293ee, v11\n" \
"ds_read_b128 v[44:47], v25 offset:1024\n" \
"v_fma_f32 v40, v40, s58, v11\n" \
"v_fma_f32 v41, v41, s58, v11\n" \
"v_exp_f32 v40, v40\n" \
"v_exp_f32 v41, v41\n" \
"v_fma_f32 v42, v42, s58, v11\n" \
"v_fma_f32 v43, v43, s58, v11\n" \
"v_exp_f32 v42, v42\n" \
"v_exp_f32 v43, v43\n" \
"v_cvt_pk_bf16_f32 v40, v40, v41\n" \
"v_cndmask_b32 v28, v30, v28, s[54:55]\n" \
"v_cvt_pk_bf16_f32 v41, v42, v43\n" \
"v_cndmask_b32 v29, v31, v29, s[54:55]\n" \
"v_mov_b32_dpp v42, v40 row_ror:8 row_mask:0xf bank_mask:0xf\n" \
"v_fma_f32 v28, v28, s58, v11\n" \
"v_mov_b32_dpp v43, v41 row_ror:8 row_mask:0xf bank_mask:0xf\n" \
"v_fma_f32 v29, v29, s58, v11\n" \
"v_exp_f32 v28, v28\n" \
"s_waitcnt lgkmcnt(0)\n" \
"v_mfma_f32_16x16x32_bf16 v[32:35], v[56:59], v[40:43], 0\n" \
"v_exp_f32 v29, v29\n" \
"v_mfma_f32_16x16x32_bf16 v[36:39], v[44:47], v[40:43], 0\n" \
"ds_read_b128 v[88:91], v7 offset:8192\n" \
"v_cvt_pk_bf16_f32 v28, v28, v29\n" \
"v_cvt_pk_bf16_f32 v60, v104, v105\n" \
"v_cvt_pk_bf16_f32 v61, v106, v107\n" \
"v_mov_b32_dpp v29, v28 row_ror:8 row_mask:0xf bank_mask:0xf\n" \
"v_cvt_pk_bf16_f32 v62, v108, v109\n" \
"v_cvt_pk_bf16_f32 v63, v110, v111\n" \
"v_mfma_f32_16x16x16_bf16 v[32:35], v[60:61], v[28:29], v[32:35]\n" \
"ds_read_b128 v[92:95], v7 offset:9216\n" \
"v_mfma_f32_16x16x16_bf16 v[36:39], v[62:63], v[28:29], v[36:39]\n" \
"ds_read_b128 v[96:99], v7 offset:10240\n" \
"v_mfma_f32_16x16x32_bf16 v[104:107], v[40:43], v[16:19], 0\n" \
"ds_read_b128 v[100:103], v7 offset:11264\n" \
"v_mov_b64 v[108:109], 0\n" \
"v_mov_b64 v[110:111], 0\n" \
"s_nop 0\n" \
"v_cvt_pk_bf16_f32 v44, v32, v33\n" \
"v_cvt_pk_bf16_f32 v45, v34, v35\n" \
"v_cvt_pk_bf16_f32 v46, v36, v37\n" \
"v_cvt_pk_bf16_f32 v47, v38, v39\n" \
"ds_write_b128 v5, v[44:47] offset:20480\n" \
"v_mfma_f32_16x16x16_bf16 v[104:107], v[28:29], v[20:21], v[104:107]\n" \
"v_mov_b64 v[28:29], 0\n" \
"v_mov_b64 v[30:31], 0\n" \
"v_mov_b32 v22, v22\n" \
"s_nop 0\n" \
"v_mov_b32 v22, v22\n" \
"s_nop 0\n" \
"v_mov_b32 v22, v22\n" \
"s_nop 0\n" \
"v_rcp_f32 v104, v104\n" \
"v_rcp_f32 v105, v105\n" \
"v_rcp_f32 v106, v106\n" \
"v_rcp_f32 v107, v107\n" \
"s_waitcnt lgkmcnt(0)\n" \
"s_barrier\n" \
"ds_read_b128 v[40:43], v6 offset:20480\n" \
"ds_read_b128 v[44:47], v6 offset:20992\n" \
"ds_read_b128 v[56:59], v6 offset:21504\n" \
"ds_read_b128 v[32:35], v6 offset:22016\n" \
"s_waitcnt lgkmcnt(3)\n" \
"v_smfmac_f32_16x16x64_bf16 v[28:31], v[40:43], v[48:55], v1\n" \
"ds_read_b128 v[112:115], v7 offset:12288\n" \
"s_waitcnt lgkmcnt(3)\n" \
"v_smfmac_f32_16x16x64_bf16 v[28:31], v[44:47], v[64:71], v1\n" \
"ds_read_b128 v[116:119], v7 offset:13312\n" \
"s_waitcnt lgkmcnt(3)\n" \
"v_smfmac_f32_16x16x64_bf16 v[28:31], v[56:59], v[72:79], v1\n" \
"ds_read_b128 v[120:123], v7 offset:14336\n" \
"s_waitcnt lgkmcnt(3)\n" \
"v_smfmac_f32_16x16x64_bf16 v[28:31], v[32:35], v[80:87], v1\n" \
"ds_read_b128 v[124:127], v7 offset:15360\n" \
"v_smfmac_f32_16x16x64_bf16 v[108:111], v[40:43], v[88:95], v1\n" \
"s_memtime s[86:87]\n" \
"v_smfmac_f32_16x16x64_bf16 v[108:111], v[44:47], v[96:103], v1\n" \
"v_pk_mul_f32 v[28:29], v[28:29], v[104:105]\n" \
"v_pk_mul_f32 v[30:31], v[30:31], v[106:107]\n" \
"v_cvt_pk_bf16_f32 v48, v28, v29\n" \
"v_cvt_pk_bf16_f32 v49, v30, v31\n" \
"buffer_store_dwordx2 v[48:49], v9, s[36:39], s47 offen sc1\n" \
"s_waitcnt lgkmcnt(0)\n" \
"v_smfmac_f32_16x16x64_bf16 v[108:111], v[56:59], v[112:119], v1\n" \
"s_add_u32 s67, s47, 0x40000\n" \
"v_smfmac_f32_16x16x64_bf16 v[108:111], v[32:35], v[120:127], v1\n" \
"v_mov_b32 v15, v10\n" \
"v_xor_b32 v24, 0x8000, v24\n" \
"s_sub_u32 s62, s60, 0x2000\n" \
"s_and_b32 s62, s62, 0x7fff\n" \
"s_add_u32 s62, s62, 0xe000\n" \
"v_add_u32 v25, s62, v8\n" \
"s_nop 1\n" \
"v_pk_mul_f32 v[108:109], v[108:109], v[104:105]\n" \
"v_pk_mul_f32 v[110:111], v[110:111], v[106:107]\n" \
"v_cvt_pk_bf16_f32 v50, v108, v109\n" \
"v_cvt_pk_bf16_f32 v51, v110, v111\n" \
"buffer_store_dwordx2 v[50:51], v9, s[36:39], s67 offen sc1\n" \
"s_cmp_lg_u32 s65, 0\n" \
"s_cbranch_scc1 .Lch_part1_%=\n" \
"ds_read_b64_tr_b16 v[60:61], v4 offset:16\n" \
"ds_read_b64_tr_b16 v[62:63], v4 offset:528\n" \
"ds_read_b128 v[64:67], v24 offset:0\n" \
"ds_read_b128 v[68:71], v24 offset:4096\n" \
"ds_read_b128 v[72:75], v24 offset:1024\n" \
"ds_read_b128 v[76:79], v24 offset:5120\n" \
"ds_read_b128 v[80:83], v24 offset:2048\n" \
"ds_read_b128 v[84:87], v24 offset:6144\n" \
"ds_read_b128 v[88:91], v24 offset:3072\n" \
"ds_read_b128 v[92:95], v24 offset:7168\n" \
"s_waitcnt lgkmcnt(8)\n" \
"s_mov_b64 exec, s[54:55]\n" \
"buffer_store_dwordx4 v[60:63], v5, s[36:39], s52 offen sc1\n" \
"s_mov_b64 exec, -1\n" \
"s_branch .Lch_shadow_%=\n" \
".Lch_part1_%=:\n" \
"ds_read_b128 v[64:67], v24 offset:0\n" \
"ds_read_b128 v[68:71], v24 offset:4096\n" \
"ds_read_b128 v[72:75], v24 offset:1024\n" \
"ds_read_b128 v[76:79], v24 offset:5120\n" \
"ds_read_b128 v[80:83], v24 offset:2048\n" \
"ds_read_b128 v[84:87], v24 offset:6144\n" \
"ds_read_b128 v[88:91], v24 offset:3072\n" \
"ds_read_b128 v[92:95], v24 offset:7168\n" \
"buffer_store_dwordx4 v[60:63], v0, s[36:39], s53 offen sc1\n" \
".Lch_shadow_%=:\n" \
"s_cmp_eq_u32 s44, 0\n" \
"s_cbranch_scc1 .Lch_first_%=\n" \
".Lch_shadow2_%=:\n" \
"s_bitcmp1_b32 s63, 0\n" \
"s_cbranch_scc0 .Lch_even_%=\n" \
"s_add_u32 s44, s44, 1\n" \
"s_add_u32 s45, s45, 0x4000\n" \
"s_add_u32 s46, s46, 0x4000\n" \
"s_and_b32 s52, s48, 0x7fffff\n" \
"s_add_u32 s52, s52, 0x8000000\n" \
"s_xor_b32 s52, s52, 0x2000\n" \
"s_mov_b32 s48, s50\n" \
"s_lshl_b32 s67, s63, 10\n" \
"s_add_u32 s53, s52, s67\n" \
"s_add_u32 s53, s53, 0x1000\n" \
"s_add_u32 s50, s50, 0x80000\n" \
"s_bitset1_b32 s50, 13\n" \
"s_bfe_u32 s67, s50, 0x10015\n" \
"s_lshr_b32 s67, s95, s67\n" \
"s_and_b32 s67, s67, 1\n" \
"s_lshl_b32 s67, s67, 13\n" \
"s_xor_b32 s50, s50, s67\n" \
"s_add_u32 s51, s50, 0x2000000\n" \
"s_xor_b32 s90, s90, 0x8000\n" \
"s_add_u32 s60, s60, 0x2000\n" \
"s_and_b32 s60, s60, 0x7fff\n" \
"s_add_u32 s93, s99, s60\n" \
"s_mov_b64 s[56:57], -1\n" \
"s_cmp_eq_u32 s44, 63\n" \
"s_cselect_b64 s[30:31], -1, 0\n" \
"s_add_u32 s49, s49, 0x4000\n" \
"s_add_u32 s94, s94, 0x80000\n" \
"s_and_b32 s94, s94, 0x1fffff\n" \
"s_add_u32 s47, s94, s92\n" \
"s_mov_b32 s26, s59\n" \
"s_mov_b32 s59, 0\n" \
"s_cmp_eq_u32 s63, 3\n" \
"s_cbranch_scc1 .Lch_loopw_%=\n" \
"s_waitcnt vmcnt(7)\n" \
"s_add_u32 s67, s44, 3\n" \
"s_mov_b32 s59, 0\n" \
".Lch_kvcn_%=:\n" \
"v_readlane_b32 s91, v26, 2\n" \
"v_readlane_b32 s62, v26, 3\n" \
"s_min_u32 s91, s91, s62\n" \
"s_add_u32 s91, s91, 18\n" \
"v_readlane_b32 s62, v26, 0\n" \
"s_cmp_eq_u32 s62, 64\n" \
"s_cselect_b32 s62, 0x100, s62\n" \
"s_min_u32 s91, s91, s62\n" \
"v_readlane_b32 s62, v26, 1\n" \
"s_cmp_eq_u32 s62, 64\n" \
"s_cselect_b32 s62, 0x100, s62\n" \
"s_min_u32 s91, s91, s62\n" \
"s_cmp_ge_u32 s91, s67\n" \
"s_cbranch_scc1 .Lch_kvokn_%=\n" \
"s_sleep 1\n" \
"buffer_load_dwordx2 v[26:27], v14, s[36:39], 0 offen sc1\n" \
"s_waitcnt vmcnt(0)\n" \
"s_add_u32 s59, s59, 1\n" \
"s_cmp_lt_u32 s59, 0x40000\n" \
"s_cbranch_scc1 .Lch_kvcn_%=\n" \
".Lch_kvokn_%=:\n" \
"s_mov_b32 s59, 0\n" \
".Lch_loopw_%=:\n" \
"s_waitcnt lgkmcnt(0)\n" \
"s_branch .Lch_loopb_%=\n" \
".Lch_even_%=:\n" \
"s_waitcnt lgkmcnt(0)\n" \
"s_sub_u32 s74, s86, s72\n" \
"s_lshl_b32 s74, s74, 4\n" \
"s_sub_u32 s74, s74, s29\n" \
"s_mov_b32 s73, s29\n" \
"s_memtime s[42:43]\n" \
"s_memrealtime s[80:81]\n" \
"s_add_u32 s44, s44, 1\n" \
"s_add_u32 s45, s45, 0x4000\n" \
"s_add_u32 s46, s46, 0x4000\n" \
"s_memrealtime s[82:83]\n" \
"s_and_b32 s52, s48, 0x7fffff\n" \
"s_add_u32 s52, s52, 0x8000000\n" \
"s_xor_b32 s52, s52, 0x2000\n" \
"s_memrealtime s[84:85]\n" \
"s_mov_b32 s48, s50\n" \
"s_lshl_b32 s67, s63, 10\n" \
"s_add_u32 s53, s52, s67\n" \
"s_memrealtime s[86:87]\n" \
"s_add_u32 s53, s53, 0x1000\n" \
"s_add_u32 s50, s50, 0x80000\n" \
"s_bitset1_b32 s50, 13\n" \
"s_memrealtime s[88:89]\n" \
"s_bfe_u32 s67, s50, 0x10015\n" \
"s_lshr_b32 s67, s95, s67\n" \
"s_and_b32 s67, s67, 1\n" \
"s_memrealtime s[96:97]\n" \
"s_lshl_b32 s67, s67, 13\n" \
"s_xor_b32 s50, s50, s67\n" \
"s_add_u32 s51, s50, 0x2000000\n" \
"s_memrealtime s[40:41]\n" \
"s_xor_b32 s90, s90, 0x8000\n" \
"s_add_u32 s60, s60, 0x2000\n" \
"s_and_b32 s60, s60, 0x7fff\n" \
"s_add_u32 s93, s99, s60\n" \
"s_mov_b64 s[56:57], -1\n" \
"s_cmp_eq_u32 s44, 63\n" \
"s_cselect_b64 s[30:31], -1, 0\n" \
"s_add_u32 s49, s49, 0x4000\n" \
"s_add_u32 s94, s94, 0x80000\n" \
"s_and_b32 s94, s94, 0x1fffff\n" \
"s_add_u32 s47, s94, s92\n" \
"s_mov_b32 s26, s59\n" \
"s_mov_b32 s59, 0\n" \
"s_mov_b32 s62, 0\n" \
"s_waitcnt lgkmcnt(0)\n" \
"s_cmp_eq_u32 s82, s80\n" \
"s_addc_u32 s62, s62, 0\n" \
"s_cmp_eq_u32 s84, s80\n" \
"s_addc_u32 s62, s62, 0\n" \
"s_cmp_eq_u32 s86, s80\n" \
"s_addc_u32 s62, s62, 0\n" \
"s_cmp_eq_u32 s88, s80\n" \
"s_addc_u32 s62, s62, 0\n" \
"s_cmp_eq_u32 s96, s80\n" \
"s_addc_u32 s62, s62, 0\n" \
"s_cmp_eq_u32 s40, s80\n" \
"s_addc_u32 s62, s62, 0\n" \
"s_lshl4_add_u32 s42, s62, s42\n" \
"s_mul_i32 s43, s80, 382\n" \
"s_lshl_b32 s42, s42, 4\n" \
"s_sub_u32 s29, s43, s42\n" \
"s_cmp_eq_u32 s63, 2\n" \
"s_cbranch_scc1 .Lch_keep_%=\n" \
"s_add_u32 s74, s74, s29\n" \
"s_sub_u32 s62, s74, s28\n" \
"s_ashr_i32 s62, s62, 1\n" \
"s_min_i32 s62, s62, 2400\n" \
"s_max_i32 s62, s62, -1600\n" \
"s_add_u32 s28, s28, s62\n" \
"s_add_u32 s98, s29, s28\n" \
"s_and_b32 s98, s98, -2\n" \
"s_branch .Lch_loopb_%=\n" \
".Lch_keep_%=:\n" \
"s_waitcnt vmcnt(7)\n" \
".Lch_stchk_%=:\n" \
"v_cmp_eq_u32 vcc, 0xffa5ffa5, v27\n" \
"s_and_b32 s78, vcc_lo, 15\n" \
"v_cmp_eq_u32 vcc, 0xffa5ffa5, v26\n" \
"s_or_b32 vcc_lo, vcc_lo, s78\n" \
"s_cmp_lg_u64 vcc, 0\n" \
"s_cbranch_scc1 .Lch_stpoll_%=\n" \
".Lch_stok_%=:\n" \
"v_readlane_b32 s75, v27, 0\n" \
"v_readlane_b32 s34, v27, 1\n" \
"v_readlane_b32 s35, v27, 2\n" \
"v_readlane_b32 s33, v27, 3\n" \
"s_lshr_b32 s27, s35, 16\n" \
"s_and_b32 s35, s35, 0xffff\n" \
"v_subrev_u32 v26, s75, v26\n" \
"s_add_u32 s78, s34, 8320\n" \
"v_cmp_lt_u32 vcc, s78, v26\n" \
"v_max_u32_dpp v26, v26, v26 row_ror:1 row_mask:0xf bank_mask:0xf\n" \
"s_nop 1\n" \
"v_max_u32_dpp v26, v26, v26 row_ror:2 row_mask:0xf bank_mask:0xf\n" \
"s_nop 1\n" \
"v_max_u32_dpp v26, v26, v26 row_ror:4 row_mask:0xf bank_mask:0xf\n" \
"s_nop 1\n" \
"v_max_u32_dpp v26, v26, v26 row_ror:8 row_mask:0xf bank_mask:0xf\n" \
"s_nop 0\n" \
"v_readlane_b32 s67, v26, 0\n" \
"v_readlane_b32 s62, v26, 16\n" \
"s_max_u32 s67, s67, s62\n" \
"v_readlane_b32 s62, v26, 32\n" \
"s_max_u32 s67, s67, s62\n" \
"v_readlane_b32 s62, v26, 48\n" \
"s_max_u32 s67, s67, s62\n" \
"s_sub_u32 s35, s35, 8\n" \
"s_max_i32 s35, s35, 0\n" \
"s_sub_u32 s27, s27, 8\n" \
"s_max_i32 s27, s27, 0\n" \
"s_cmp_eq_u64 vcc, -1\n" \
"s_cselect_b32 s62, 640, 0\n" \
"s_cselect_b32 s79, 0, 768\n" \
"s_cmp_lg_u32 s33, 0\n" \
"s_cselect_b32 s62, 0, s62\n" \
"s_cselect_b32 s79, 0, s79\n" \
"s_add_u32 s27, s27, s62\n" \
"s_lshr_b32 s33, s33, 1\n" \
"s_add_u32 s33, s33, 10400\n" \
"s_add_u32 s78, s34, 768\n" \
"s_min_u32 s78, s78, s67\n" \
"s_cmp_eq_u64 vcc, 0\n" \
"s_cselect_b32 s79, 0, s79\n" \
"s_cselect_b32 s34, s78, s34\n" \
"s_cselect_b32 s33, 0, s33\n" \
"s_mov_b32 s62, s33\n" \
"s_cselect_b32 s78, 0, 5600\n" \
"s_add_u32 s67, s67, s78\n" \
"s_add_u32 s35, s35, s79\n" \
"s_min_u32 s35, s35, 0x2000\n" \
"s_min_u32 s27, s27, 0x2000\n" \
"s_add_u32 s67, s67, s75\n" \
"s_add_u32 s67, s67, s27\n" \
"s_add_u32 s75, s67, s62\n" \
"s_add_u32 s75, s75, s35\n" \
"s_lshl4_add_u32 s75, s61, s75\n" \
"s_and_b32 s75, s75, -2\n" \
"s_lshl_b32 s62, s27, 16\n" \
"s_or_b32 s62, s62, s35\n" \
"v_mov_b32 v22, s75\n" \
"v_writelane_b32 v22, s34, 1\n" \
"v_writelane_b32 v22, s62, 2\n" \
"v_writelane_b32 v22, s33, 3\n" \
"buffer_store_dword v22, v12, s[36:39], s49 offen sc1\n" \
"v_add_u32 v14, 0x4000, v14\n" \
"s_sub_u32 s62, s29, s73\n" \
"s_min_i32 s62, s62, 0x1400\n" \
"s_max_i32 s62, s62, 0xffffec00\n" \
"s_mul_i32 s62, s62, 6\n" \
"s_ashr_i32 s62, s62, 5\n" \
"s_sub_u32 s75, s75, s62\n" \
"s_cbranch_vccz .Lch_rel_%=\n" \
".Lch_lw_%=:\n" \
"s_memrealtime s[78:79]\n" \
"s_waitcnt lgkmcnt(0)\n" \
"s_mul_i32 s78, s78, 382\n" \
"s_sub_u32 s62, s75, s78\n" \
"s_cmp_gt_i32 s62, 10400\n" \
"s_cbranch_scc1 .Lch_lw_%=\n" \
"s_memtime s[42:43]\n" \
"s_memrealtime s[80:81]\n" \
"s_nop 2\n" \
"s_memrealtime s[82:83]\n" \
"s_nop 2\n" \
"s_memrealtime s[84:85]\n" \
"s_nop 2\n" \
"s_memrealtime s[86:87]\n" \
"s_nop 2\n" \
"s_memrealtime s[88:89]\n" \
"s_nop 2\n" \
"s_memrealtime s[96:97]\n" \
"s_nop 2\n" \
"s_memrealtime s[40:41]\n" \
"s_mov_b32 s62, 0\n" \
"s_waitcnt lgkmcnt(0)\n" \
"s_cmp_eq_u32 s82, s80\n" \
"s_addc_u32 s62, s62, 0\n" \
"s_cmp_eq_u32 s84, s80\n" \
"s_addc_u32 s62, s62, 0\n" \
"s_cmp_eq_u32 s86, s80\n" \
"s_addc_u32 s62, s62, 0\n" \
"s_cmp_eq_u32 s88, s80\n" \
"s_addc_u32 s62, s62, 0\n" \
"s_cmp_eq_u32 s96, s80\n" \
"s_addc_u32 s62, s62, 0\n" \
"s_cmp_eq_u32 s40, s80\n" \
"s_addc_u32 s62, s62, 0\n" \
"s_lshl4_add_u32 s42, s62, s42\n" \
"s_mul_i32 s43, s80, 382\n" \
"s_lshl_b32 s42, s42, 4\n" \
"s_sub_u32 s29, s43, s42\n" \
".Lch_rel_%=:\n" \
"s_sub_u32 s74, s75, s29\n" \
".Lch_cwa_%=:\n" \
"s_memtime s[78:79]\n" \
"s_nop 0\n" \
"s_nop 1\n" \
"s_nop 0\n" \
"s_nop 1\n" \
"s_nop 0\n" \
"s_waitcnt lgkmcnt(0)\n" \
"s_nop 0\n" \
"s_lshl_b32 s78, s78, 4\n" \
"s_sub_u32 s62, s74, s78\n" \
"s_ashr_i32 s62, s62, 4\n" \
"s_cmp_gt_i32 s62, 384\n" \
"s_cbranch_scc1 .Lch_cwa_%=\n" \
"s_nop 0\n" \
"s_max_i32 s62, s62, 0\n" \
"s_and_b32 s62, s62, 0xfffc\n" \
"s_nop 0\n" \
"s_getpc_b64 s[78:79]\n" \
"s_sub_u32 s62, 408, s62\n" \
"s_add_u32 s78, s78, s62\n" \
"s_addc_u32 s79, s79, 0\n" \
"s_nop 0\n" \
"s_setpc_b64 s[78:79]\n" \
"s_nop 0\n" \
"s_nop 0\n" \
"s_nop 0\n" \
"s_nop 0\n" \
"s_nop 0\n" \
"s_nop 0\n" \
"s_nop 0\n" \
"s_nop 0\n" \
"s_nop 0\n" \
"s_nop 0\n" \
"s_nop 0\n" \
"s_nop 0\n" \
"s_nop 0\n" \
"s_nop 0\n" \
"s_nop 0\n" \
"s_nop 0\n" \
"s_nop 0\n" \
"s_nop 0\n" \
"s_nop 0\n" \
"s_nop 0\n" \
"s_nop 0\n" \
"s_nop 0\n" \
"s_nop 0\n" \
"s_nop 0\n" \
"s_nop 0\n" \
"s_nop 0\n" \
"s_nop 0\n" \
"s_nop 0\n" \
"s_nop 0\n" \
"s_nop 0\n" \
"s_nop 0\n" \
"s_nop 0\n" \
"s_nop 0\n" \
"s_nop 0\n" \
"s_nop 0\n" \
"s_nop 0\n" \
"s_nop 0\n" \
"s_nop 0\n" \
"s_nop 0\n" \
"s_nop 0\n" \
"s_nop 0\n" \
"s_nop 0\n" \
"s_nop 0\n" \
"s_nop 0\n" \
"s_nop 0\n" \
"s_nop 0\n" \
"s_nop 0\n" \
"s_nop 0\n" \
"s_nop 0\n" \
"s_nop 0\n" \
"s_nop 0\n" \
"s_nop 0\n" \
"s_nop 0\n" \
"s_nop 0\n" \
"s_nop 0\n" \
"s_nop 0\n" \
"s_nop 0\n" \
"s_nop 0\n" \
"s_nop 0\n" \
"s_nop 0\n" \
"s_nop 0\n" \
"s_nop 0\n" \
"s_nop 0\n" \
"s_nop 0\n" \
"s_nop 0\n" \
"s_nop 0\n" \
"s_nop 0\n" \
"s_nop 0\n" \
"s_nop 0\n" \
"s_nop 0\n" \
"s_nop 0\n" \
"s_nop 0\n" \
"s_nop 0\n" \
"s_nop 0\n" \
"s_nop 0\n" \
"s_nop 0\n" \
"s_nop 0\n" \
"s_nop 0\n" \
"s_nop 0\n" \
"s_nop 0\n" \
"s_nop 0\n" \
"s_nop 0\n" \
"s_nop 0\n" \
"s_nop 0\n" \
"s_nop 0\n" \
"s_nop 0\n" \
"s_nop 0\n" \
"s_nop 0\n" \
"s_nop 0\n" \
"s_nop 0\n" \
"s_nop 0\n" \
"s_nop 0\n" \
"s_nop 0\n" \
"s_nop 0\n" \
"s_nop 0\n" \
"s_nop 0\n" \
"s_branch .Lch_loopb_%=\n" \
".Lch_stpoll_%=:\n" \
"s_add_u32 s69, s69, 1\n" \
"s_add_u32 s59, s59, 1\n" \
"s_cmp_lt_u32 s59, 0x10000\n" \
"s_cbranch_scc0 .Lch_stbad_%=\n" \
"s_sleep 1\n" \
"buffer_load_dwordx2 v[26:27], v14, s[36:39], 0 offen sc1\n" \
"s_waitcnt vmcnt(0)\n" \
"s_branch .Lch_stchk_%=\n" \
".Lch_stbad_%=:\n" \
"v_add_u32 v14, 0x4000, v14\n" \
"s_branch .Lch_loopb_%=\n" \
".Lch_first_%=:\n" \
"s_waitcnt vmcnt(0) lgkmcnt(0)\n" \
"s_cmp_lg_u32 s63, 1\n" \
"s_cbranch_scc1 .Lch_first2_%=\n" \
"s_mov_b32 s59, 0\n" \
".Lch_kvcf_%=:\n" \
"v_readlane_b32 s91, v26, 2\n" \
"v_readlane_b32 s62, v26, 3\n" \
"s_min_u32 s91, s91, s62\n" \
"s_add_u32 s91, s91, 18\n" \
"v_readlane_b32 s62, v26, 0\n" \
"s_cmp_eq_u32 s62, 64\n" \
"s_cselect_b32 s62, 0x100, s62\n" \
"s_min_u32 s91, s91, s62\n" \
"v_readlane_b32 s62, v26, 1\n" \
"s_cmp_eq_u32 s62, 64\n" \
"s_cselect_b32 s62, 0x100, s62\n" \
"s_min_u32 s91, s91, s62\n" \
"s_cmp_ge_u32 s91, 3\n" \
"s_cbranch_scc1 .Lch_kvokf_%=\n" \
"s_sleep 1\n" \
"buffer_load_dwordx2 v[26:27], v14, s[36:39], 0 offen sc1\n" \
"s_waitcnt vmcnt(0)\n" \
"s_add_u32 s59, s59, 1\n" \
"s_cmp_lt_u32 s59, 0x40000\n" \
"s_cbranch_scc1 .Lch_kvcf_%=\n" \
".Lch_kvokf_%=:\n" \
"s_mov_b32 s59, 0\n" \
".Lch_first2_%=:\n" \
"s_barrier\n" \
"s_xor_b32 m0, s90, 0x8000\n" \
"s_nop 0\n" \
"buffer_load_dwordx4 v8, s[36:39], s48 offen sc1 lds\n" \
"buffer_load_dwordx4 v8, s[36:39], s48 offen offset:1024 sc1 lds\n" \
"s_add_u32 m0, s99, 0x2000\n" \
"s_add_u32 s62, s48, 0x2000000\n" \
"buffer_load_dwordx4 v8, s[36:39], s62 offen sc1 lds\n" \
"buffer_load_dwordx4 v8, s[36:39], s62 offen offset:1024 sc1 lds\n" \
"s_mov_b32 m0, s90\n" \
"s_nop 0\n" \
"buffer_load_dwordx4 v8, s[36:39], s50 offen sc1 lds\n" \
"buffer_load_dwordx4 v8, s[36:39], s50 offen offset:1024 sc1 lds\n" \
"s_mov_b32 m0, s93\n" \
"s_nop 0\n" \
"buffer_load_dwordx4 v8, s[36:39], s51 offen sc1 lds\n" \
"buffer_load_dwordx4 v8, s[36:39], s51 offen offset:1024 sc1 lds\n" \
"s_waitcnt vmcnt(6)\n" \
"s_barrier\n" \
"ds_read_b128 v[64:67], v24 offset:0\n" \
"ds_read_b128 v[68:71], v24 offset:4096\n" \
"ds_read_b128 v[72:75], v24 offset:1024\n" \
"ds_read_b128 v[76:79], v24 offset:5120\n" \
"ds_read_b128 v[80:83], v24 offset:2048\n" \
"ds_read_b128 v[84:87], v24 offset:6144\n" \
"ds_read_b128 v[88:91], v24 offset:3072\n" \
"ds_read_b128 v[92:95], v24 offset:7168\n" \
"s_branch .Lch_shadow2_%=\n" \
".Lch_slowA_%=:\n" \
"v_mov_b32 v22, 0\n" \
"v_max3_u32 v22, v32, v33, v22\n" \
"v_max3_u32 v22, v34, v35, v22\n" \
"v_max3_u32 v22, v36, v37, v22\n" \
"v_max3_u32 v22, v38, v39, v22\n" \
"v_max3_u32 v22, v40, v41, v22\n" \
"v_max3_u32 v22, v42, v43, v22\n" \
"v_max3_u32 v22, v44, v45, v22\n" \
"v_max3_u32 v22, v46, v47, v22\n" \
"v_max3_u32 v22, v48, v49, v22\n" \
"v_max3_u32 v22, v50, v51, v22\n" \
"v_max3_u32 v22, v52, v53, v22\n" \
"v_max3_u32 v22, v54, v55, v22\n" \
"v_max3_u32 v22, v56, v57, v22\n" \
"v_max3_u32 v22, v58, v59, v22\n" \
"v_max3_u32 v22, v60, v61, v22\n" \
"v_max3_u32 v22, v62, v63, v22\n" \
"v_cmp_eq_u32 vcc, 0xffa5ffa5, v22\n" \
"s_and_b64 vcc, vcc, s[56:57]\n" \
"s_cbranch_vccnz .Lch_retA_%=\n" \
"s_cmp_eq_u32 s44, 63\n" \
"s_cbranch_scc1 .Lch_last_%=\n" \
"s_branch .Lch_okA_%=\n" \
".Lch_retA_%=:\n" \
"s_add_u32 s62, s73, 27200\n" \
"v_mov_b32 v11, s62\n" \
"buffer_store_dword v11, v10, s[36:39], s49 offen sc1\n" \
"v_mov_b32 v15, 0x7f000000\n" \
"s_add_u32 s68, s68, 1\n" \
"s_add_u32 s59, s59, 1\n" \
"s_cmp_lt_u32 s59, 0x4000\n" \
"s_cselect_b64 s[56:57], -1, 0\n" \
"s_waitcnt lgkmcnt(0)\n" \
"s_branch .Lch_loop_%=\n" \
".Lch_last_%=:\n" \
"s_waitcnt lgkmcnt(0)\n" \
"s_barrier\n" \
"v_smfmac_f32_16x16x64_bf16 v[108:111], v[48:51], v[200:207], v1\n" \
"v_smfmac_f32_16x16x64_bf16 v[104:107], v[52:55], v[208:215], v1\n" \
"v_smfmac_f32_16x16x64_bf16 v[108:111], v[52:55], v[216:223], v1\n" \
"v_smfmac_f32_16x16x64_bf16 v[104:107], v[56:59], v[224:231], v1\n" \
"v_smfmac_f32_16x16x64_bf16 v[108:111], v[56:59], v[232:239], v1\n" \
"v_smfmac_f32_16x16x64_bf16 v[104:107], v[60:63], v[240:247], v1\n" \
"v_smfmac_f32_16x16x64_bf16 v[108:111], v[60:63], v[248:255], v1\n" \
"s_nop 7\n" \
"v_cvt_pk_bf16_f32 v60, v104, v105\n" \
"v_cvt_pk_bf16_f32 v61, v106, v107\n" \
"v_cvt_pk_bf16_f32 v62, v108, v109\n" \
"v_cvt_pk_bf16_f32 v63, v110, v111\n" \
"s_cmp_eq_u32 s65, 0\n" \
"s_cbranch_scc1 .Lch_pkc_%=\n" \
"buffer_store_dwordx4 v[60:63], v0, s[36:39], s53 offen sc1\n" \
"s_branch .Lch_pdc_%=\n" \
".Lch_pkc_%=:\n" \
"ds_read_b64_tr_b16 v[60:61], v4 offset:16\n" \
"ds_read_b64_tr_b16 v[62:63], v4 offset:528\n" \
"s_waitcnt lgkmcnt(0)\n" \
"s_mov_b64 exec, s[54:55]\n" \
"buffer_store_dwordx4 v[60:63], v5, s[36:39], s52 offen sc1\n" \
"s_mov_b64 exec, -1\n" \
".Lch_pdc_%=:\n" \
"s_waitcnt vmcnt(0) lgkmcnt(0)\n" \
"s_barrier\n" \
"v_mov_b32 v22, 64\n" \
"buffer_store_dword v22, v13, s[36:39], 0 offen sc1\n" \
"s_waitcnt vmcnt(0)\n" \
".Lch_end_%=:\n" \
"s_waitcnt vmcnt(0) lgkmcnt(0)\n" \
"s_barrier\n" \
"s_mov_b64 exec, -1\n" \
"v_mbcnt_lo_u32_b32 v0, -1, 0\n" \
"v_mbcnt_hi_u32_b32 v0, -1, v0\n" \
"s_lshl_b32 s62, s19, 6\n" \
"v_or_b32 v0, s62, v0\n" \
"s_mov_b64 s[0:1], s[24:25]\n" \
"s_lshl_b32 s2, s95, 8\n" \
"s_add_u32 s2, s2, s23\n" \
"s_add_u32 s2, s2, 0x100\n" \
"s_getpc_b64 s[78:79]\n" \
"s_add_u32 s78, s78, k_main@rel32@lo+4\n" \
"s_addc_u32 s79, s79, k_main@rel32@hi+12\n" \
"s_setpc_b64 s[78:79]\n"
#define RED_ASM \
"s_cmp_lg_u32 s4, 0\n" \
"s_cbranch_scc1 .Lrd_end_%=\n" \
"s_mov_b64 s[36:37], %0\n" \
"s_and_b32 s37, s37, 0xffff\n" \
"s_mov_b32 s38, 0xe000000\n" \
"s_mov_b32 s39, 0x20000\n" \
"s_mov_b32 s66, %1\n" \
"s_mov_b32 s95, %2\n" \
"s_mov_b32 s61, %3\n" \
"s_mov_b32 s91, %4\n" \
"s_mov_b64 s[76:77], %5\n" \
"v_mbcnt_lo_u32_b32 v32, -1, 0\n" \
"v_mbcnt_hi_u32_b32 v32, -1, v32\n" \
"v_lshrrev_b32 v33, 4, v32\n" \
"v_and_b32 v34, 15, v32\n" \
"v_lshlrev_b32 v0, 4, v32\n" \
"v_cmp_gt_u32 vcc, 8, v34\n" \
"s_mov_b64 s[54:55], vcc\n" \
"v_and_b32 v47, 1, v34\n" \
"v_lshl_add_u32 v48, v33, 1, v47\n" \
"v_lshrrev_b32 v49, 1, v34\n" \
"v_lshlrev_b32 v10, 7, v48\n" \
"v_lshl_add_u32 v10, v49, 4, v10\n" \
"v_lshlrev_b32 v26, 4, v34\n" \
"v_and_b32 v27, 1, v33\n" \
"v_lshl_add_u32 v26, v27, 3, v26\n" \
"v_lshrrev_b32 v27, 1, v33\n" \
"v_lshl_add_u32 v26, v27, 2, v26\n" \
"v_mov_b32 v50, 0x3f80\n" \
"v_lshlrev_b32 v47, 4, v47\n" \
"v_lshlrev_b32 v50, v47, v50\n" \
"v_cmp_eq_u32 vcc, 0, v49\n" \
"s_nop 1\n" \
"v_cndmask_b32 v12, 0, v50, vcc\n" \
"v_cmp_eq_u32 vcc, 1, v49\n" \
"s_nop 1\n" \
"v_cndmask_b32 v13, 0, v50, vcc\n" \
"v_cmp_eq_u32 vcc, 2, v49\n" \
"s_nop 1\n" \
"v_cndmask_b32 v14, 0, v50, vcc\n" \
"v_cmp_eq_u32 vcc, 3, v49\n" \
"s_nop 1\n" \
"v_cndmask_b32 v15, 0, v50, vcc\n" \
"s_lshr_b32 s92, s66, 1\n" \
"s_lshl_b32 s92, s92, 14\n" \
"s_and_b32 s62, s66, 1\n" \
"s_lshl_b32 s62, s62, 12\n" \
"s_add_u32 s92, s92, s62\n" \
"s_lshr_b32 s62, s95, 1\n" \
"s_lshl_b32 s62, s62, 13\n" \
"s_add_u32 s92, s92, s62\n" \
"s_add_u32 s92, s92, 0x200000\n" \
"s_mov_b32 s94, 0\n" \
"s_and_b32 s62, s95, 1\n" \
"s_lshl_b32 s62, s62, 13\n" \
"s_lshl_b32 s49, s66, 7\n" \
"s_add_u32 s49, s49, s62\n" \
"s_add_u32 s49, s49, 0x4000\n" \
"v_mov_b32 v16, 0xffa5ffa5\n" \
"v_mov_b32 v17, 0xffa5ffa5\n" \
"v_mov_b32 v18, 0xffa5ffa5\n" \
"v_mov_b32 v19, 0xffa5ffa5\n" \
"s_and_b32 s94, s94, 0x1fffff\n" \
"s_add_u32 s47, s94, s92\n" \
"s_mov_b32 s44, 0\n" \
"s_mov_b32 s69, 0\n" \
"s_mov_b32 s59, 0\n" \
"s_mov_b32 s68, 0\n" \
"s_mov_b32 s99, 0\n" \
"v_lshlrev_b32 v1, 7, v32\n" \
"s_lshl_b32 s62, s66, 7\n" \
"s_sub_u32 s45, s49, s62\n" \
"s_add_u32 s45, s45, 0xfc000\n" \
"s_lshl_b32 s90, %6, 4\n" \
"s_mov_b32 s71, 1\n" \
"s_mov_b32 s58, 0\n" \
"s_branch .Lrd_rds_%=\n" \
".Lrd_next_%=:\n" \
"s_memrealtime s[78:79]\n" \
"s_waitcnt lgkmcnt(0)\n" \
"s_mul_i32 s78, s78, 382\n" \
"s_sub_u32 s62, s74, s78\n" \
"s_cmp_gt_i32 s62, 0\n" \
"s_cbranch_scc1 .Lrd_next_%=\n" \
"s_mov_b32 s71, 1\n" \
".Lrd_rds_%=:\n" \
"v_lshlrev_b32 v22, 3, v0\n" \
"buffer_load_dwordx2 v[112:113], v22, s[36:39], s45 offen sc1\n" \
"s_memtime s[42:43]\n" \
"s_memrealtime s[80:81]\n" \
"s_nop 2\n" \
"s_memrealtime s[82:83]\n" \
"s_nop 2\n" \
"s_memrealtime s[84:85]\n" \
"s_nop 2\n" \
"s_memrealtime s[86:87]\n" \
"s_nop 2\n" \
"s_memrealtime s[88:89]\n" \
"s_nop 2\n" \
"s_memrealtime s[96:97]\n" \
"s_nop 2\n" \
"s_memrealtime s[40:41]\n" \
"s_mov_b32 s62, 0\n" \
"s_waitcnt lgkmcnt(0)\n" \
"s_cmp_eq_u32 s82, s80\n" \
"s_addc_u32 s62, s62, 0\n" \
"s_cmp_eq_u32 s84, s80\n" \
"s_addc_u32 s62, s62, 0\n" \
"s_cmp_eq_u32 s86, s80\n" \
"s_addc_u32 s62, s62, 0\n" \
"s_cmp_eq_u32 s88, s80\n" \
"s_addc_u32 s62, s62, 0\n" \
"s_cmp_eq_u32 s96, s80\n" \
"s_addc_u32 s62, s62, 0\n" \
"s_cmp_eq_u32 s40, s80\n" \
"s_addc_u32 s62, s62, 0\n" \
"s_lshl4_add_u32 s42, s62, s42\n" \
"s_mul_i32 s43, s80, 382\n" \
"s_lshl_b32 s42, s42, 4\n" \
"s_sub_u32 s98, s43, s42\n" \
"s_waitcnt vmcnt(0)\n" \
"v_cmp_eq_u32 vcc, 0xffa5ffa5, v113\n" \
"s_and_b32 s78, vcc_lo, 15\n" \
"v_cmp_eq_u32 vcc, 0xffa5ffa5, v112\n" \
"s_or_b32 vcc_lo, vcc_lo, s78\n" \
"s_cmp_lg_u64 vcc, 0\n" \
"s_cbranch_scc1 .Lrd_miss_%=\n" \
"v_readlane_b32 s75, v113, 0\n" \
"v_readlane_b32 s34, v113, 1\n" \
"v_readlane_b32 s35, v113, 2\n" \
"v_readlane_b32 s33, v113, 3\n" \
"s_lshr_b32 s27, s35, 16\n" \
"s_and_b32 s35, s35, 0xffff\n" \
"v_subrev_u32 v112, s75, v112\n" \
"s_add_u32 s78, s34, 8320\n" \
"v_cmp_lt_u32 vcc, s78, v112\n" \
"v_max_u32_dpp v112, v112, v112 row_ror:1 row_mask:0xf bank_mask:0xf\n" \
"s_nop 1\n" \
"v_max_u32_dpp v112, v112, v112 row_ror:2 row_mask:0xf bank_mask:0xf\n" \
"s_nop 1\n" \
"v_max_u32_dpp v112, v112, v112 row_ror:4 row_mask:0xf bank_mask:0xf\n" \
"s_nop 1\n" \
"v_max_u32_dpp v112, v112, v112 row_ror:8 row_mask:0xf bank_mask:0xf\n" \
"s_nop 0\n" \
"v_readlane_b32 s67, v112, 0\n" \
"v_readlane_b32 s62, v112, 16\n" \
"s_max_u32 s67, s67, s62\n" \
"v_readlane_b32 s62, v112, 32\n" \
"s_max_u32 s67, s67, s62\n" \
"v_readlane_b32 s62, v112, 48\n" \
"s_max_u32 s67, s67, s62\n" \
"s_sub_u32 s35, s35, 8\n" \
"s_max_i32 s35, s35, 0\n" \
"s_sub_u32 s27, s27, 8\n" \
"s_max_i32 s27, s27, 0\n" \
"s_cmp_eq_u64 vcc, -1\n" \
"s_cselect_b32 s62, 640, 0\n" \
"s_cselect_b32 s79, 0, 768\n" \
"s_cmp_lg_u32 s33, 0\n" \
"s_cselect_b32 s62, 0, s62\n" \
"s_cselect_b32 s79, 0, s79\n" \
"s_add_u32 s27, s27, s62\n" \
"s_lshr_b32 s33, s33, 1\n" \
"s_add_u32 s33, s33, 10400\n" \
"s_add_u32 s78, s34, 768\n" \
"s_min_u32 s78, s78, s67\n" \
"s_cmp_eq_u64 vcc, 0\n" \
"s_cselect_b32 s79, 0, s79\n" \
"s_cselect_b32 s34, s78, s34\n" \
"s_cselect_b32 s33, 0, s33\n" \
"s_mov_b32 s62, s33\n" \
"s_cselect_b32 s78, 0, 5600\n" \
"s_add_u32 s67, s67, s78\n" \
"s_add_u32 s35, s35, s79\n" \
"s_min_u32 s35, s35, 0x2000\n" \
"s_min_u32 s27, s27, 0x2000\n" \
"s_add_u32 s67, s67, s75\n" \
"s_add_u32 s67, s67, s27\n" \
"s_add_u32 s75, s67, s62\n" \
"s_add_u32 s75, s75, s35\n" \
"s_lshl4_add_u32 s75, s61, s75\n" \
"s_add_u32 s74, s75, s34\n" \
"s_sub_u32 s74, s74, s90\n" \
"s_mov_b32 s59, 0\n" \
"s_mov_b32 s71, 0\n" \
"s_lshl4_add_u32 s67, s91, s67\n" \
"s_add_u32 s67, s67, s58\n" \
"s_sub_u32 s62, s98, s70\n" \
"s_min_i32 s62, s62, 0x1400\n" \
"s_max_i32 s62, s62, 0xffffec00\n" \
"s_mul_i32 s62, s62, 10\n" \
"s_ashr_i32 s62, s62, 5\n" \
"s_mov_b32 s70, s98\n" \
"s_add_u32 s98, s98, s62\n" \
"s_cbranch_vccz .Lrd_near_%=\n" \
".Lrd_lw_%=:\n" \
"s_memrealtime s[78:79]\n" \
"s_waitcnt lgkmcnt(0)\n" \
"s_mul_i32 s78, s78, 382\n" \
"s_sub_u32 s62, s67, s78\n" \
"s_cmp_gt_i32 s62, 10400\n" \
"s_cbranch_scc1 .Lrd_lw_%=\n" \
"s_memtime s[42:43]\n" \
"s_memrealtime s[80:81]\n" \
"s_nop 2\n" \
"s_memrealtime s[82:83]\n" \
"s_nop 2\n" \
"s_memrealtime s[84:85]\n" \
"s_nop 2\n" \
"s_memrealtime s[86:87]\n" \
"s_nop 2\n" \
"s_memrealtime s[88:89]\n" \
"s_nop 2\n" \
"s_memrealtime s[96:97]\n" \
"s_nop 2\n" \
"s_memrealtime s[40:41]\n" \
"s_mov_b32 s62, 0\n" \
"s_waitcnt lgkmcnt(0)\n" \
"s_cmp_eq_u32 s82, s80\n" \
"s_addc_u32 s62, s62, 0\n" \
"s_cmp_eq_u32 s84, s80\n" \
"s_addc_u32 s62, s62, 0\n" \
"s_cmp_eq_u32 s86, s80\n" \
"s_addc_u32 s62, s62, 0\n" \
"s_cmp_eq_u32 s88, s80\n" \
"s_addc_u32 s62, s62, 0\n" \
"s_cmp_eq_u32 s96, s80\n" \
"s_addc_u32 s62, s62, 0\n" \
"s_cmp_eq_u32 s40, s80\n" \
"s_addc_u32 s62, s62, 0\n" \
"s_lshl4_add_u32 s42, s62, s42\n" \
"s_mul_i32 s43, s80, 382\n" \
"s_lshl_b32 s42, s42, 4\n" \
"s_sub_u32 s98, s43, s42\n" \
".Lrd_near_%=:\n" \
"s_sub_u32 s67, s67, s98\n" \
".Lrd_cwa_%=:\n" \
"s_memtime s[78:79]\n" \
"s_nop 0\n" \
"s_nop 1\n" \
"s_nop 0\n" \
"s_nop 1\n" \
"s_nop 0\n" \
"s_waitcnt lgkmcnt(0)\n" \
"s_nop 0\n" \
"s_lshl_b32 s78, s78, 4\n" \
"s_sub_u32 s62, s67, s78\n" \
"s_ashr_i32 s62, s62, 4\n" \
"s_cmp_gt_i32 s62, 384\n" \
"s_cbranch_scc1 .Lrd_cwa_%=\n" \
"s_nop 0\n" \
"s_max_i32 s62, s62, 0\n" \
"s_and_b32 s62, s62, 0xfffc\n" \
"s_nop 0\n" \
"s_getpc_b64 s[78:79]\n" \
"s_sub_u32 s62, 408, s62\n" \
"s_add_u32 s78, s78, s62\n" \
"s_addc_u32 s79, s79, 0\n" \
"s_nop 0\n" \
"s_setpc_b64 s[78:79]\n" \
"s_nop 0\n" \
"s_nop 0\n" \
"s_nop 0\n" \
"s_nop 0\n" \
"s_nop 0\n" \
"s_nop 0\n" \
"s_nop 0\n" \
"s_nop 0\n" \
"s_nop 0\n" \
"s_nop 0\n" \
"s_nop 0\n" \
"s_nop 0\n" \
"s_nop 0\n" \
"s_nop 0\n" \
"s_nop 0\n" \
"s_nop 0\n" \
"s_nop 0\n" \
"s_nop 0\n" \
"s_nop 0\n" \
"s_nop 0\n" \
"s_nop 0\n" \
"s_nop 0\n" \
"s_nop 0\n" \
"s_nop 0\n" \
"s_nop 0\n" \
"s_nop 0\n" \
"s_nop 0\n" \
"s_nop 0\n" \
"s_nop 0\n" \
"s_nop 0\n" \
"s_nop 0\n" \
"s_nop 0\n" \
"s_nop 0\n" \
"s_nop 0\n" \
"s_nop 0\n" \
"s_nop 0\n" \
"s_nop 0\n" \
"s_nop 0\n" \
"s_nop 0\n" \
"s_nop 0\n" \
"s_nop 0\n" \
"s_nop 0\n" \
"s_nop 0\n" \
"s_nop 0\n" \
"s_nop 0\n" \
"s_nop 0\n" \
"s_nop 0\n" \
"s_nop 0\n" \
"s_nop 0\n" \
"s_nop 0\n" \
"s_nop 0\n" \
"s_nop 0\n" \
"s_nop 0\n" \
"s_nop 0\n" \
"s_nop 0\n" \
"s_nop 0\n" \
"s_nop 0\n" \
"s_nop 0\n" \
"s_nop 0\n" \
"s_nop 0\n" \
"s_nop 0\n" \
"s_nop 0\n" \
"s_nop 0\n" \
"s_nop 0\n" \
"s_nop 0\n" \
"s_nop 0\n" \
"s_nop 0\n" \
"s_nop 0\n" \
"s_nop 0\n" \
"s_nop 0\n" \
"s_nop 0\n" \
"s_nop 0\n" \
"s_nop 0\n" \
"s_nop 0\n" \
"s_nop 0\n" \
"s_nop 0\n" \
"s_nop 0\n" \
"s_nop 0\n" \
"s_nop 0\n" \
"s_nop 0\n" \
"s_nop 0\n" \
"s_nop 0\n" \
"s_nop 0\n" \
"s_nop 0\n" \
"s_nop 0\n" \
"s_nop 0\n" \
"s_nop 0\n" \
"s_nop 0\n" \
"s_nop 0\n" \
"s_nop 0\n" \
"s_nop 0\n" \
"s_nop 0\n" \
"s_nop 0\n" \
"s_nop 0\n" \
"s_nop 0\n" \
"s_nop 0\n" \
".Lrd_poll_%=:\n" \
"buffer_load_dwordx4 v[32:35], v0, s[36:39], s47 offen offset:0 sc1\n" \
"buffer_load_dwordx4 v[36:39], v0, s[36:39], s47 offen offset:1024 sc1\n" \
"buffer_load_dwordx4 v[40:43], v0, s[36:39], s47 offen offset:2048 sc1\n" \
"buffer_load_dwordx4 v[44:47], v0, s[36:39], s47 offen offset:3072 sc1\n" \
"s_waitcnt vmcnt(3)\n" \
"v_mfma_f32_16x16x32_bf16 v[96:99], v[32:35], v[12:15], 0\n" \
"s_waitcnt vmcnt(2)\n" \
"v_mfma_f32_16x16x32_bf16 v[96:99], v[36:39], v[12:15], v[96:99]\n" \
"s_waitcnt vmcnt(1)\n" \
"v_mfma_f32_16x16x32_bf16 v[96:99], v[40:43], v[12:15], v[96:99]\n" \
"s_waitcnt vmcnt(0)\n" \
"v_mfma_f32_16x16x32_bf16 v[96:99], v[44:47], v[12:15], v[96:99]\n" \
"s_nop 7\n" \
"v_permlane32_swap_b32 v96, v98\n" \
"v_permlane32_swap_b32 v97, v99\n" \
"v_pk_add_f32 v[96:97], v[96:97], v[98:99]\n" \
"v_cmp_u_f32 vcc, v96, v97\n" \
"v_cvt_pk_bf16_f32 v100, v96, v97\n" \
"v_cvt_pk_bf16_f32 v102, v96, v97\n" \
"s_cbranch_vccnz .Lrd_slowB_%=\n" \
".Lrd_redok_%=:\n" \
"s_nop 0\n" \
"v_permlane16_swap_b32 v100, v102\n" \
"v_mov_b32 v101, v100\n" \
"v_mov_b32 v103, v102\n" \
"s_nop 0\n" \
"v_permlane32_swap_b32 v100, v101\n" \
"v_permlane32_swap_b32 v102, v103\n" \
"s_mov_b64 exec, 0xff\n" \
"buffer_store_dwordx4 v[100:103], v0, s[36:39], s49 offen sc1\n" \
"s_mov_b64 exec, -1\n" \
"s_or_b32 s62, s59, s71\n" \
"s_cmp_eq_u32 s62, 0\n" \
"s_cbranch_scc1 .Lrd_adv_%=\n" \
"s_mov_b32 s59, 0\n" \
".Lrd_late_%=:\n" \
"s_add_u32 s59, s59, 1\n" \
"s_cmp_lt_u32 s59, 0x10000\n" \
"s_cbranch_scc0 .Lrd_adv_%=\n" \
"v_lshlrev_b32 v22, 3, v0\n" \
"buffer_load_dwordx2 v[112:113], v22, s[36:39], s45 offen sc1\n" \
"s_memtime s[42:43]\n" \
"s_memrealtime s[80:81]\n" \
"s_nop 2\n" \
"s_memrealtime s[82:83]\n" \
"s_nop 2\n" \
"s_memrealtime s[84:85]\n" \
"s_nop 2\n" \
"s_memrealtime s[86:87]\n" \
"s_nop 2\n" \
"s_memrealtime s[88:89]\n" \
"s_nop 2\n" \
"s_memrealtime s[96:97]\n" \
"s_nop 2\n" \
"s_memrealtime s[40:41]\n" \
"s_mov_b32 s62, 0\n" \
"s_waitcnt lgkmcnt(0)\n" \
"s_cmp_eq_u32 s82, s80\n" \
"s_addc_u32 s62, s62, 0\n" \
"s_cmp_eq_u32 s84, s80\n" \
"s_addc_u32 s62, s62, 0\n" \
"s_cmp_eq_u32 s86, s80\n" \
"s_addc_u32 s62, s62, 0\n" \
"s_cmp_eq_u32 s88, s80\n" \
"s_addc_u32 s62, s62, 0\n" \
"s_cmp_eq_u32 s96, s80\n" \
"s_addc_u32 s62, s62, 0\n" \
"s_cmp_eq_u32 s40, s80\n" \
"s_addc_u32 s62, s62, 0\n" \
"s_lshl4_add_u32 s42, s62, s42\n" \
"s_mul_i32 s43, s80, 382\n" \
"s_lshl_b32 s42, s42, 4\n" \
"s_sub_u32 s98, s43, s42\n" \
"s_waitcnt vmcnt(0)\n" \
"v_cmp_eq_u32 vcc, 0xffa5ffa5, v113\n" \
"s_and_b32 s78, vcc_lo, 15\n" \
"v_cmp_eq_u32 vcc, 0xffa5ffa5, v112\n" \
"s_or_b32 vcc_lo, vcc_lo, s78\n" \
"s_cmp_lg_u64 vcc, 0\n" \
"s_cbranch_scc1 .Lrd_late_%=\n" \
"v_readlane_b32 s75, v113, 0\n" \
"v_readlane_b32 s34, v113, 1\n" \
"v_readlane_b32 s35, v113, 2\n" \
"v_readlane_b32 s33, v113, 3\n" \
"s_lshr_b32 s27, s35, 16\n" \
"s_and_b32 s35, s35, 0xffff\n" \
"v_subrev_u32 v112, s75, v112\n" \
"s_add_u32 s78, s34, 8320\n" \
"v_cmp_lt_u32 vcc, s78, v112\n" \
"v_max_u32_dpp v112, v112, v112 row_ror:1 row_mask:0xf bank_mask:0xf\n" \
"s_nop 1\n" \
"v_max_u32_dpp v112, v112, v112 row_ror:2 row_mask:0xf bank_mask:0xf\n" \
"s_nop 1\n" \
"v_max_u32_dpp v112, v112, v112 row_ror:4 row_mask:0xf bank_mask:0xf\n" \
"s_nop 1\n" \
"v_max_u32_dpp v112, v112, v112 row_ror:8 row_mask:0xf bank_mask:0xf\n" \
"s_nop 0\n" \
"v_readlane_b32 s67, v112, 0\n" \
"v_readlane_b32 s62, v112, 16\n" \
"s_max_u32 s67, s67, s62\n" \
"v_readlane_b32 s62, v112, 32\n" \
"s_max_u32 s67, s67, s62\n" \
"v_readlane_b32 s62, v112, 48\n" \
"s_max_u32 s67, s67, s62\n" \
"s_sub_u32 s35, s35, 8\n" \
"s_max_i32 s35, s35, 0\n" \
"s_sub_u32 s27, s27, 8\n" \
"s_max_i32 s27, s27, 0\n" \
"s_cmp_eq_u64 vcc, -1\n" \
"s_cselect_b32 s62, 640, 0\n" \
"s_cselect_b32 s79, 0, 768\n" \
"s_cmp_lg_u32 s33, 0\n" \
"s_cselect_b32 s62, 0, s62\n" \
"s_cselect_b32 s79, 0, s79\n" \
"s_add_u32 s27, s27, s62\n" \
"s_lshr_b32 s33, s33, 1\n" \
"s_add_u32 s33, s33, 10400\n" \
"s_add_u32 s78, s34, 768\n" \
"s_min_u32 s78, s78, s67\n" \
"s_cmp_eq_u64 vcc, 0\n" \
"s_cselect_b32 s79, 0, s79\n" \
"s_cselect_b32 s34, s78, s34\n" \
"s_cselect_b32 s33, 0, s33\n" \
"s_mov_b32 s62, s33\n" \
"s_cselect_b32 s78, 0, 5600\n" \
"s_add_u32 s67, s67, s78\n" \
"s_add_u32 s35, s35, s79\n" \
"s_min_u32 s35, s35, 0x2000\n" \
"s_min_u32 s27, s27, 0x2000\n" \
"s_add_u32 s67, s67, s75\n" \
"s_add_u32 s67, s67, s27\n" \
"s_add_u32 s75, s67, s62\n" \
"s_add_u32 s75, s75, s35\n" \
"s_lshl4_add_u32 s75, s61, s75\n" \
"s_add_u32 s74, s75, s34\n" \
"s_sub_u32 s74, s74, s90\n" \
".Lrd_adv_%=:\n" \
"s_mov_b32 s58, 0\n" \
"s_sleep 12\n" \
"buffer_store_dwordx4 v[16:19], v0, s[36:39], s47 offen offset:0 sc1\n" \
"buffer_store_dwordx4 v[16:19], v0, s[36:39], s47 offen offset:1024 sc1\n" \
"buffer_store_dwordx4 v[16:19], v0, s[36:39], s47 offen offset:2048 sc1\n" \
"buffer_store_dwordx4 v[16:19], v0, s[36:39], s47 offen offset:3072 sc1\n" \
"s_add_u32 s44, s44, 1\n" \
"s_add_u32 s49, s49, 0x4000\n" \
"s_add_u32 s45, s45, 0x4000\n" \
"s_add_u32 s94, s94, 0x80000\n" \
"s_and_b32 s94, s94, 0x1fffff\n" \
"s_add_u32 s47, s94, s92\n" \
"s_mov_b32 s59, 0\n" \
"s_cmp_lt_u32 s44, 63\n" \
"s_cbranch_scc1 .Lrd_next_%=\n" \
"s_branch .Lrd_fin_%=\n" \
".Lrd_slowB_%=:\n" \
"s_mov_b64 exec, -1\n" \
"v_mov_b32 v22, 0\n" \
"v_max3_u32 v22, v32, v33, v22\n" \
"v_max3_u32 v22, v34, v35, v22\n" \
"v_max3_u32 v22, v36, v37, v22\n" \
"v_max3_u32 v22, v38, v39, v22\n" \
"v_max3_u32 v22, v40, v41, v22\n" \
"v_max3_u32 v22, v42, v43, v22\n" \
"v_max3_u32 v22, v44, v45, v22\n" \
"v_max3_u32 v22, v46, v47, v22\n" \
"v_cmp_eq_u32 vcc, 0xffa5ffa5, v22\n" \
"s_cbranch_vccz .Lrd_redok_%=\n" \
"s_or_b32 s62, s59, s71\n" \
"s_cmp_eq_u32 s62, 0\n" \
"s_addc_u32 s69, s69, 0\n" \
"s_bcnt1_i32_b64 s62, vcc\n" \
"s_cmp_lt_u32 s62, 24\n" \
"s_cbranch_scc1 .Lrd_pollon_%=\n" \
"s_or_b32 s62, s58, s33\n" \
"s_cmp_lg_u32 s62, 0\n" \
"s_cbranch_scc1 .Lrd_pollon_%=\n" \
"s_mov_b32 s58, 7200\n" \
"s_mov_b32 s71, 1\n" \
"s_branch .Lrd_rds_%=\n" \
".Lrd_pollon_%=:\n" \
"s_add_u32 s59, s59, 1\n" \
"s_cmp_lt_u32 s59, 0x10000\n" \
"s_cbranch_scc0 .Lrd_redok_%=\n" \
"s_mov_b64 exec, -1\n" \
"s_sleep 1\n" \
"s_branch .Lrd_poll_%=\n" \
".Lrd_miss_%=:\n" \
"s_cmp_eq_u32 s59, 0\n" \
"s_addc_u32 s68, s68, 0\n" \
"s_add_u32 s59, s59, 1\n" \
"s_cmp_lt_u32 s59, 0x4000\n" \
"s_cbranch_scc1 .Lrd_rds_%=\n" \
"s_mov_b32 s59, 0\n" \
"s_branch .Lrd_poll_%=\n" \
".Lrd_fin_%=:\n" \
"s_sleep 16\n" \
"s_waitcnt vmcnt(0)\n" \
".Lrd_end_%=:\n" \
"s_waitcnt vmcnt(0) lgkmcnt(0)\n" \
"s_barrier\n" \
"s_mov_b64 exec, -1\n" \
"v_mbcnt_lo_u32_b32 v0, -1, 0\n" \
"v_mbcnt_hi_u32_b32 v0, -1, v0\n" \
"s_lshl_b32 s62, s4, 6\n" \
"v_or_b32 v0, s62, v0\n" \
"s_mov_b64 s[0:1], s[24:25]\n" \
"s_lshl_b32 s2, s19, 8\n" \
"s_add_u32 s2, s2, s23\n" \
"s_add_u32 s2, s2, 0x100\n" \
"s_getpc_b64 s[78:79]\n" \
"s_add_u32 s78, s78, k_main@rel32@lo+4\n" \
"s_addc_u32 s79, s79, k_main@rel32@hi+12\n" \
"s_setpc_b64 s[78:79]\n"
#define CHAIN_CLOBBERS "v255", "a255", "s99", "vcc", "scc", "memory"
__device__ __forceinline__ float row_sum16(float v) {
  v += u2f(__builtin_amdgcn_update_dpp(0u, f2u(v), 0x121, 0xf, 0xf, false)); v += u2f(__builtin_amdgcn_update_dpp(0u, f2u(v), 0x122, 0xf, 0xf, false));
  v += u2f(__builtin_amdgcn_update_dpp(0u, f2u(v), 0x124, 0xf, 0xf, false)); v += u2f(__builtin_amdgcn_update_dpp(0u, f2u(v), 0x128, 0xf, 0xf, false));
  return v;
}
__device__ __forceinline__ float wave_sum(float v) {
  v = row_sum16(v);
  { unsigned x = f2u(v); u32x2 r = __builtin_amdgcn_permlane16_swap(x, x, false, false); unsigned a = r.x, b = r.y; v = u2f(a) + u2f(b); }
  { unsigned x = f2u(v); u32x2 r = __builtin_amdgcn_permlane32_swap(x, x, false, false); unsigned a = r.x, b = r.y; v = u2f(a) + u2f(b); }
  return v;
}
__device__ __forceinline__ void ln8(float (&f)[8]) {
  const float mean = wave_sum(((f[0] + f[1]) + (f[2] + f[3])) + ((f[4] + f[5]) + (f[6] + f[7]))) * (1.0f / D_);
  float q = 0.f;
#pragma unroll
  for (int j = 0; j < 8; ++j) { f[j] -= mean; q += f[j] * f[j]; }
  const float r = __builtin_amdgcn_rsqf(wave_sum(q) * (1.0f / D_) + 1e-5f);
#pragma unroll
  for (int j = 0; j < 8; ++j) f[j] *= r;
}
__device__ __forceinline__ void touch_role(const Params& p, int what) {
  const int lane = threadIdx.x & 63;
  unsigned x = 0;
  if (what == 0) {
    const rsrc_t rws = mk_rsrc(p.ws, WS_END);
    x ^= __builtin_amdgcn_raw_buffer_load_b32(rws, lane * 0x200000u + 0x1FF000u, 0, 0);
    x ^= __builtin_amdgcn_raw_buffer_load_b32(rws, (64 + lane) * 0x200000u + 0x1FF000u, 0, 0);
    x ^= __builtin_amdgcn_raw_buffer_load_b32(mk_rsrc(p.w1, S_ * F_ * 2), lane * 0xFFF00u, 0, 0);
    x ^= __builtin_amdgcn_raw_buffer_load_b32(mk_rsrc(p.w2, S_ * F_ * 2), lane * 0xFFF00u, 0, 0);
    x ^= __builtin_amdgcn_raw_buffer_load_b32(mk_rsrc(p.x, S_ * D_ * 2), lane * 0xFFF00u, 0, 0);
    x ^= __builtin_amdgcn_raw_buffer_load_b32(mk_rsrc(p.out, S_ * D_ * 2), lane * 0xFFF00u, 0, 0);
  } else {
    unsigned lo, hi;
    const unsigned len = CODE_LEN;
    asm volatile("s_getpc_b64 s[40:41]\n\ts_add_u32 %0, s40, k_main@rel32@lo+4\n\ts_addc_u32 %1, s41, k_main@rel32@hi+12"
                 : "=s"(lo), "=s"(hi) :: "scc", "s40", "s41");
    const rsrc_t rc = mk_rsrc((const void*)(((unsigned long long)hi << 32) | lo), len);
    unsigned v[4];
#pragma unroll
    for (int u = 0; u < 4; ++u) v[u] = __builtin_amdgcn_raw_buffer_load_b32(rc, lane * 128 + (u * 3 + what - 1) * 8192, 0, 0);
#pragma unroll
    for (int u = 0; u < 4; ++u) x ^= v[u];
  }
  asm volatile("" :: "v"(x));
}
__device__ __forceinline__ unsigned wpre_issue(const Params& p, int gx, int k) {
  const int lane = threadIdx.x & 63;
  const rsrc_t rq = mk_rsrc(p.Wq, 512 * 4096 * 2), ro = mk_rsrc(p.Wo, 4096 * 512 * 2);
  const unsigned vo = (lane >> 4) * 8192 + (lane & 15) * 128;
  unsigned v[4];
#pragma unroll
  for (int u = 0; u < 2; ++u) v[u] = __builtin_amdgcn_raw_buffer_load_b32(rq, vo, ((1 - u) * 64 + k) * 32768 + gx * 2048, 0);
#pragma unroll
  for (int u = 0; u < 2; ++u) v[2 + u] = __builtin_amdgcn_raw_buffer_load_b32(ro, lane * 128, gx * 0x100000 + (u * 64 + k) * 8192, 0);
  __builtin_amdgcn_sched_barrier(0);
  return v[0] ^ v[1] ^ v[2] ^ v[3];
}
__device__ __forceinline__ unsigned setup_role(Params& p, int r, unsigned long long tag, LAS unsigned char* L) {
  const int tid = threadIdx.x, lane = tid & 63, w = __builtin_amdgcn_readfirstlane(tid >> 6);
  const rsrc_t rws = mk_rsrc(p.ws, WS_END);
  const u32x4 S4 = {SENT, SENT, SENT, SENT}, Z4 = {0u, 0u, 0u, 0u};
  if (r >= 48 && (r & 3) == 0) {
    const int which = (r - 48) >> 2;
    if (tid == 0) {
      unsigned best = 0xffffu;
      gu32* q = (gu32*)(p.ws + ((which >> 1) ? PART_OFF + 0x180000 : MEMX_OFF + 0xA0000) + (which & 1) * 8192);
      for (int rep = 0; rep < 4; ++rep) {
        unsigned long long t0, t1; unsigned v = SENT;
        asm volatile("s_memtime %0\n s_waitcnt lgkmcnt(0)\n global_store_dword %3, %2, off sc1\n s_waitcnt vmcnt(0)\n s_memtime %1\n s_waitcnt lgkmcnt(0)\n"
                     : "=&s"(t0), "=&s"(t1), "+v"(v) : "v"(q) : "memory");
        best = min(best, (unsigned)(t1 - t0));
      }
      __hip_atomic_store((gu64*)(p.ws + PROBE_OFF + which * 128), (tag << 16) | best, RLX_AGENT);
    }
    BAR();
  }
  if (r >= 44 && r < 48) {
    const int which = r - 44;
#pragma unroll
    for (int i = 0; i < (int)(FLAG_SPAN / 4 / 4096); ++i) st_sc1(Z4, rws, tid * 16 + i * 4096, FLAG_OFF + which * (FLAG_SPAN / 4));
    WAIT_V(0);
    BAR();
    if (tid == 0) __hip_atomic_store((gu64*)(p.ws + PROBE_OFF + (4 + which) * 128), tag << 16, RLX_AGENT);
  }
  u32x4 m0v = S4;
  if ((tid & 127) < 16) {
    const int t = r >> 3, blk = (t & 1) | ((t & 2) << 1) | ((t & 4) >> 1);
    const u32x4* src = (const u32x4*)(p.mem0 + ((r & 1) * 4 + (tid & 3)) * D_ + blk * 64 + ((r >> 1) & 3) * 16);
    const u32x4 a = src[0], b = src[1];
    const unsigned sel = (tid & 4) ? 0x07060302u : 0x05040100u;
    m0v = u32x4{__builtin_amdgcn_perm(a.y, a.x, sel), __builtin_amdgcn_perm(a.w, a.z, sel), __builtin_amdgcn_perm(b.y, b.x, sel), __builtin_amdgcn_perm(b.w, b.z, sel)};
    m0v.x = m0v.x == SENT ? (SENT | 0x00400040u) : m0v.x; m0v.y = m0v.y == SENT ? (SENT | 0x00400040u) : m0v.y; m0v.z = m0v.z == SENT ? (SENT | 0x00400040u) : m0v.z; m0v.w = m0v.w == SENT ? (SENT | 0x00400040u) : m0v.w;
  }
#pragma unroll
  for (int i = 0; i < 4; ++i) st_sc1(S4, rws, tid * 16, PART_OFF + (i >> 1) * 0x80000 + (r >> 1) * 16384 + (i & 1) * 8192 + (r & 1) * 4096);
  st_sc1(tid < 128 ? m0v : S4, rws, (tid >> 7) * (STMP_OFF - MEMX_OFF) + ((tid >> 3) & 15) * 8192 + (tid & 7) * 16, MEMX_OFF + r * 128);
  WAIT_V(0);
  BAR();
  if (tid == 0) __hip_atomic_store((gu64*)(p.ws + INIT_OFF + r * 128), tag, RLX_AGENT);
  TS(6);
  if ((r >> 2) == 10 && !(p.roles & 128)) touch_role(p, w);
  unsigned keep = 0;
  if (!(p.roles & 256)) keep = wpre_issue(p, r & 3, (r >> 2) * 4 + w);
  const unsigned sel = boot_wait(p, tag);
  p.fsel = ~sel;
  asm volatile("" :: "v"(keep));
#pragma unroll
  for (int i = 0; i < 2; ++i) {
    const unsigned sl = (tid >> 3) + 32 * i, o = sl >= 8 ? sl * 16384 + (tid & 7) * 16 : 0x7f000000u;
    st_sc1(S4, rws, o, MEMX_OFF + (sel & 1) * 8192 + r * 128);
    st_sc1(S4, rws, o, STMP_OFF + (sel & 1) * 8192 + r * 128);
    st_sc1(S4, rws, tid * 16, PART_OFF + (2 + i) * 0x80000 + (r >> 1) * 16384 + (sel >> 1) * 8192 + (r & 1) * 4096);
  }
  WAIT_V(0);
  BAR();
  return sel;
}
__device__ __forceinline__ void init_wait(const Params& p, unsigned long long tag) {
  gu64* f = (gu64*)(p.ws + INIT_OFF + (threadIdx.x & 63) * 128);
  for (unsigned spins = 0; spins < (1u << 20); ++spins) {
    if (__all(__hip_atomic_load(f, RLX_AGENT) == tag)) break;
    __builtin_amdgcn_s_sleep(2);
  }
  __builtin_amdgcn_fence(__ATOMIC_ACQUIRE, "wavefront");
}
__device__ __forceinline__ unsigned kvm_touch(const Params& p, int u) {
  const int lane = threadIdx.x & 63;
  return __builtin_amdgcn_raw_buffer_load_b32(mk_rsrc(p.ws, WS_END), (lane < 16 || lane >= 32) ? lane * 128 : 0x7f000000u, spread(p, KVM_OFF, u * 8192) ^ 0x2000u, AUX_SC1);
}
__device__ __forceinline__ void ln_role(Params& p, int j, unsigned long long tag, LAS unsigned char* L) {
  const int tid = threadIdx.x, lane = tid & 63, w = __builtin_amdgcn_readfirstlane(tid >> 6);
  const rsrc_t rws = mk_rsrc(p.ws, WS_END);
  u32x4 xn[8];
#pragma unroll
  for (int i = 0; i < 8; ++i) xn[i] = *(const u32x4*)(p.x + (size_t)(j * 32 + w * 8 + i) * D_ + lane * 8);
#pragma nounroll
  for (int k = 0; k < 4; ++k) {
    LAS unsigned char* T = L + k * 32768;
    float f[8][8];
#pragma unroll
    for (int i = 0; i < 8; ++i) {
      const u32x4 v = xn[i];
      f[i][0] = bflo(v.x); f[i][1] = bfhi(v.x); f[i][2] = bflo(v.y); f[i][3] = bfhi(v.y); f[i][4] = bflo(v.z); f[i][5] = bfhi(v.z); f[i][6] = bflo(v.w); f[i][7] = bfhi(v.w);
      xn[i] = *(const u32x4*)(p.x + (size_t)(((j + 16 * k + 16) & 63) * 32 + w * 8 + i) * D_ + lane * 8);
    }
#pragma unroll
    for (int i = 0; i < 8; ++i) ln8(f[i]);
#pragma unroll
    for (int i = 0; i < 8; ++i) {
      const int row = w * 8 + i;
      *(LAS u32x4*)(T + (row >> 4) * 16384 + lane * 256 + (((row & 15) ^ (lane & 7)) * 16)) = u32x4{pk_bf16(f[i][0], f[i][1]), pk_bf16(f[i][2], f[i][3]), pk_bf16(f[i][4], f[i][5]), pk_bf16(f[i][6], f[i][7])};
    }
    WAIT_L(0);
    BAR();
#pragma unroll
    for (int i = 0; i < 8; ++i) {
      const int e = i * 256 + tid;
      st_sc1(*(LAS u32x4*)(T + (e >> 4) * 256 + (((e & 15) ^ ((e >> 4) & 7)) * 16)), rws, e * 16, XN_OFF + (j + 16 * k) * 32768);
    }
    if (k == 0) p.fsel = ~boot_wait(p, tag);
    if (k == 0 || k == 3) {
      WAIT_V(0);
      BAR();
      if (tid == 0) __hip_atomic_store(flagp(p, FL_X + j), (unsigned)k + 1u, RLX_AGENT);
      if (k == 0) TS(1);
    }
  }
}

__device__ __forceinline__ void stage_w(desc_t rs, unsigned base, unsigned rstride, unsigned lds, int w, int lane) {
  const int g = lane >> 4, c = lane & 15;
#pragma unroll
  for (int i = 0; i < 8; ++i) {
    const int row = w * 32 + i * 4 + g;
    const int phi = g | (((i >> 1) & 1) << 2);
    dma16<false>(rs, (unsigned)g * rstride + (unsigned)((c ^ (phi << 1)) * 16), base + (unsigned)(w * 32 + i * 4) * rstride, lds + w * 8192 + i * 1024);
    (void)row;
  }
}
template <int MODE>
__device__ __forceinline__ void qkv_role(Params& p, unsigned long long tag, const u16* W, int h0, unsigned outb, int flag, LAS unsigned char* L) {
  constexpr int NU = MODE == 2 ? 2 : 1;
  const int tid = threadIdx.x, lane = tid & 63, w = __builtin_amdgcn_readfirstlane(tid >> 6);
  const int g = lane >> 4, q = (lane >> 2) & 3, pp = lane & 3;
  const desc_t r0 = mk_desc(W, 512 * 4096 * 2), dws = mk_desc(p.ws, WS_END);
  const rsrc_t rws = mk_rsrc(p.ws, WS_END);
  auto fetch1 = [&](int seg, int i) { dma16<true>(dws, lane * 16, XN_OFF + (seg & 63) * 32768 + w * 8192 + i * 1024, (seg & 3) * 32768 + w * 8192 + i * 1024); };
  u32x4 Wf[NU][2][16];
#pragma unroll
  for (int ch = 0; ch < 4; ++ch) stage_w(r0, ch * 128 * 8192 + h0 * 256, 8192, ch * 32768, w, lane);
#pragma unroll
  for (int ch = 0; ch < 4 * NU; ++ch) {
    if (ch <= 4 * NU - 4) WAIT_V(24); else if (ch == 4 * NU - 3) WAIT_V(16); else if (ch == 4 * NU - 2) WAIT_V(8); else WAIT_V(0);
    BAR();
    LAS unsigned char* base = L + (ch & 3) * 32768;
    const int phi = q | ((g & 1) << 2);
#pragma unroll
    for (int tl = 0; tl < 2; ++tl)
#pragma unroll
      for (int kk = 0; kk < 4; ++kk) {
        LAS unsigned char* a = base + (32 * kk + 8 * g + q) * 256 + (((4 * w + 2 * tl + (pp >> 1)) ^ (phi << 1)) * 16) + (pp & 1) * 8;
        Wf[ch >> 2][tl][(ch & 3) * 4 + kk] = cat(lds_tr(a), lds_tr(a + 1024));
      }
    WAIT_L(0);
    BAR();
    if (ch + 4 < 4 * NU) stage_w(r0, ch * 128 * 8192 + (h0 + 1) * 256, 8192, ch * 32768, w, lane);
  }
  p.fsel = ~boot_wait(p, tag);
  unsigned fx = 0;
  wait_prog(p, lane < 16 ? FL_X + lane : -1, 1, fx, 1);
#pragma unroll
  for (int i = 0; i < 16; ++i) fetch1(i >> 3, i & 7);
  if (MODE != 2 && w == 0) init_wait(p, tag);
  TS(3);
  if (NU == 2) {
#pragma unroll
    for (int tl = 0; tl < 2; ++tl)
#pragma unroll
      for (int ks = 0; ks < 16; ++ks) asm volatile("" : "+a"(Wf[NU - 1][tl][ks]));
  }
  ACCDECL; CYC(cs);
  for (int seg = 0; seg < NSEG; ++seg) {
    CYC(c0);
    if (seg == 1) TS(6);
    if (seg == 3) TS(7);
    if (seg == 13) wait_prog(p, lane < 16 ? FL_X + lane : -1, 4, fx, 1);
    if (seg == 0) WAIT_V(8); else if (seg == 1 || seg == 3) WAIT_V(0); else if (NU == 2) WAIT_V(12); else WAIT_V(10);
    CYC(c1);
    BAR();
    CYC(c2);
    ACC(0, c1 - c0); ACC(1, c2 - c1);
    if (seg > 0 && tid == 0) __hip_atomic_store(flagp(p, flag), (unsigned)(seg == 1 || seg == 3 ? seg : seg - 1), RLX_AGENT);
    LAS u32x4* X = (LAS u32x4*)(L + (seg & 3) * 32768) + lane;
    f32x4 acc[NU][2][2];
#pragma unroll
    for (int u = 0; u < NU; ++u)
#pragma unroll
      for (int tl = 0; tl < 2; ++tl)
#pragma unroll
        for (int tt = 0; tt < 2; ++tt) acc[u][tl][tt] = f32x4{0.f, 0.f, 0.f, 0.f};
    const unsigned fs0 = pin_s(XN_OFF + ((seg + 2) & 63) * 32768 + w * 8192), fs1 = pin_s(fs0 + 4096), fl0 = pin_s(((seg + 2) & 3) * 32768 + w * 8192), fl1 = pin_s(fl0 + 4096);
    u32x4 xa[3][2];
    xa[0][0] = X[0]; xa[0][1] = X[1024]; xa[1][0] = X[64]; xa[1][1] = X[1024 + 64];
#define SB() __builtin_amdgcn_sched_barrier(0)
#define MM(u, tl, tt, x) acc[u][tl][tt] = MODE == 1 ? mfma32(x, Wf[u][tl][ks], acc[u][tl][tt]) : mfma32(Wf[u][tl][ks], x, acc[u][tl][tt])
#pragma unroll
    for (int ks = 0; ks < 16; ++ks) {
      const u32x4 x0 = xa[ks % 3][0], x1 = xa[ks % 3][1];
      MM(0, 0, 0, x0); SB();
      if (ks + 2 < 16) xa[(ks + 2) % 3][0] = X[(ks + 2) * 64];
      SB();
      MM(0, 1, 0, x0); SB();
      if (ks + 2 < 16) xa[(ks + 2) % 3][1] = X[1024 + (ks + 2) * 64];
      SB();
      MM(0, 0, 1, x1); SB();
      if (ks & 1) dma16q<true>((ks >> 1) & 3, dws, lane * 16, ks < 8 ? fs0 : fs1, ks < 8 ? fl0 : fl1);
      SB();
      MM(0, 1, 1, x1);
      if (NU == 2) { MM(NU - 1, 0, 0, x0); MM(NU - 1, 1, 0, x0); MM(NU - 1, 0, 1, x1); MM(NU - 1, 1, 1, x1); }
      SB();
    }
    CYC(c3); ACC(2, c3 - c2);
#pragma unroll
    for (int u = 0; u < NU; ++u) {
      const unsigned o = spread(p, outb, (seg * H_ + h0 + u) * 8192);
      if (MODE == 1) {
        st_sc1(pk8(acc[u][0][0], acc[u][0][1]), rws, lane * 16, o + (2 * w) * 1024);
        st_sc1(pk8(acc[u][1][0], acc[u][1][1]), rws, lane * 16, o + (2 * w + 1) * 1024);
      } else {
        st_sc1(pk8(acc[u][0][0], acc[u][1][0]), rws, lane * 16, o + w * 1024);
        st_sc1(pk8(acc[u][0][1], acc[u][1][1]), rws, lane * 16, o + (4 + w) * 1024);
      }
    }
  }
  CYC(ce); ACC(3, ce - cs); ACCOUT;
  WAIT_V(0);
  BAR();
  if (tid == 0) __hip_atomic_store(flagp(p, flag), (unsigned)NSEG, RLX_AGENT);
}

template <int N> __device__ __forceinline__ void warm_role(const Params& p, int u0) {
  const int lane = threadIdx.x & 63;
  const rsrc_t r1 = mk_rsrc(p.w1, S_ * F_ * 2), r2 = mk_rsrc(p.w2, S_ * F_ * 2);
  unsigned v[2 * N], x = 0;
#pragma unroll
  for (int u = 0; u < N; ++u) {
    v[2 * u] = __builtin_amdgcn_raw_buffer_load_b32(r1, lane * 128 + (u0 + u) * 8192, 0, 0);
    v[2 * u + 1] = __builtin_amdgcn_raw_buffer_load_b32(r2, lane * 128 + (u0 + u) * 8192, 0, 0);
  }
#pragma unroll
  for (int u = 0; u < 2 * N; ++u) x ^= v[u];
  asm volatile("" :: "v"(x));
}
template <int N, int NK> __device__ __forceinline__ void bg_warm(const Params& p, unsigned t0, int u0, int k0) {
  if (p.roles & 64) return;
  while ((unsigned)__builtin_amdgcn_s_memrealtime() - t0 < (p.pad & 255u) * 100u) __builtin_amdgcn_s_sleep(4);
  unsigned x = 0;
#pragma unroll
  for (int i = 0; i < NK; ++i) x ^= kvm_touch(p, k0 + i * 128);
  warm_role<N>(p, u0);
  asm volatile("" :: "v"(x));
}
__device__ __forceinline__ void ao_role(Params& p, unsigned long long tag, unsigned t0, int pr, int set, LAS unsigned char* L) {
  const int phase = set == 2 ? SEG3 : set, fo = (set == 2 ? FL_AC : FL_AO + set * 16) + pr;
  const int tid = threadIdx.x, lane = tid & 63, w = __builtin_amdgcn_readfirstlane(tid >> 6);
  const int g = lane >> 4, c = lane & 15, q = (lane >> 2) & 3, pp = lane & 3;
  const desc_t rwo = mk_desc(p.Wo, 4096 * 512 * 2);
  const rsrc_t rws = mk_rsrc(p.ws, WS_END);
  u32x4 Wf[8][8];
  auto stage = [&](int ch) {
#pragma unroll
    for (int i = 0; i < 8; ++i) {
      const int row = w * 8 + i;
      const int phi = (row & 3) | (((row >> 2) & 1) << 2);
      const int sw = (phi & 1) | ((phi >> 1) << 2);
      dma16<false>(rwo, (unsigned)((lane ^ sw) * 16), (unsigned)(256 * pr + 32 * ch + row) * 1024, (ch & 3) * 32768 + row * 1024);
    }
  };
#pragma unroll
  for (int ch = 0; ch < 4; ++ch) stage(ch);
#pragma unroll
  for (int ch = 0; ch < 8; ++ch) {
    if (ch < 5) WAIT_V(24); else if (ch == 5) WAIT_V(16); else if (ch == 6) WAIT_V(8); else WAIT_V(0);
    BAR();
    LAS unsigned char* base = L + (ch & 3) * 32768;
    const int phi = q | ((g & 1) << 2);
    const int sw = (phi & 1) | ((phi >> 1) << 2);
#pragma unroll
    for (int ct = 0; ct < 8; ++ct) {
      const int slot = 16 * w + 4 * (ct >> 1) + pp;
      LAS unsigned char* a = base + (4 * g + q) * 1024 + ((slot ^ sw) * 16) + (ct & 1) * 8;
      Wf[ct][ch] = cat(lds_tr(a), lds_tr(a + 16384));
    }
    WAIT_L(0);
    BAR();
    if (ch < 4) stage(ch + 4);
  }
  TS(3);
  const int hh = w >> 1, qt = w & 1, h = 2 * pr + hh;
  const float cexp = 0.08838834764831845f * 1.4426950408889634f;
  const unsigned ONE2 = 0x3F803F80u;
  const u32x4 ones = {ONE2, ONE2, ONE2, ONE2};
  const u32x4 ones_m = g < 2 ? u32x4{ONE2, ONE2, 0u, 0u} : u32x4{0u, 0u, 0u, 0u};
#pragma unroll
  for (int ct = 2; ct < 8; ++ct)
#pragma unroll
    for (int ks = 0; ks < 8; ++ks) asm volatile("" : "+a"(Wf[ct][ks]));
  const desc_t dws = mk_desc(p.ws, WS_END);
  const int fa = lane < 2 ? FL_K + 2 * pr + lane : (lane < 4 ? FL_V + 2 * pr + lane - 2 : (lane == 4 ? FL_Q + pr : -1)), fb = lane < 2 ? FL_KM + 2 * h + lane : -1;
  unsigned va = 0, vb = 0;
  auto fetch1 = [&](int seg, int slot, int i) {
    const unsigned mo = (i >> 2) == 0 ? QF_OFF : ((i >> 2) == 1 ? KF_OFF : VF_OFF);
    dma16<true>(dws, lane * 16, spread(p, mo, (seg * H_ + 2 * pr) * 8192 + 4 * w * 1024) + (i & 3) * 1024, slot * 49152 + (i >> 2) * 16384 + (4 * w + (i & 3)) * 1024);
  };
  p.fsel = ~boot_wait(p, tag);
  if (set < 2) { const int k = (pr * 2 + set) * 4 + w; bg_warm<5, 4>(p, t0, k * 5, k); TS(7); }
  wait_prog(p, fa, phase + 1, va, 4);
#pragma unroll
  for (int i = 0; i < 12; ++i) fetch1(phase, 0, i);
  volatile LAS int* lateflag = (volatile LAS int*)(L + 98304 + 16384);
  *lateflag = 0;
  WAIT_V(0);
  WAIT_L(0);
  BAR();
  ACCDECL;
  const f32x4 z = {0.f, 0.f, 0.f, 0.f};
  u32x4 Km[4], Vm[4];
  auto loadkvm = [&](int seg) {
    const unsigned kvm = spread(p, KVM_OFF, ((seg & 15) * H_ + h) * 8192) ^ 0x2000u;
#pragma unroll
    for (int ks = 0; ks < 4; ++ks) Km[ks] = ld_sc1(rws, ks * 512 + g * 128 + (c & 7) * 16, kvm);
#pragma unroll
    for (int ks = 0; ks < 4; ++ks) Vm[ks] = ld_sc1(rws, ks * 1024 + lane * 16, kvm + 4096);
  };
  bool have = false;
  int slot = 0, prev = -1;
  for (int seg = phase, nx; seg < NSEG; prev = seg, seg = nx, slot ^= 1) {
    nx = seg + 2 < SEG3 ? seg + 2 : seg + 3;
    CYC(c0);
    int late = 0;
    LAS u32x4* SQ = (LAS u32x4*)(L + slot * 49152 + hh * 8192) + lane;
    u32x4 Q[4], V[8];
    f32x4 T0 = z, T1 = z, Tm = z;
#pragma unroll
    for (int ks = 0; ks < 4; ++ks) {
      Q[ks] = SQ[(qt * 4 + ks) * 64];
      T0 = mfma32(SQ[1024 + ks * 64], Q[ks], T0); T1 = mfma32(SQ[1024 + (4 + ks) * 64], Q[ks], T1);
    }
#pragma unroll
    for (int ft = 0; ft < 8; ++ft) V[ft] = SQ[2048 + ft * 64];
    const int qi = 16 * qt + c;
    float mx = -3.0e38f;
#pragma unroll
    for (int r = 0; r < 4; ++r) {
      if (4 * g + r > qi) T0[r] = -__builtin_inff();
      if (16 + 4 * g + r > qi) T1[r] = -__builtin_inff();
      mx = fmaxf(mx, fmaxf(T0[r], T1[r]));
    }
    asm volatile("" :: "v"(mx));
    CYC(c1);
    if (seg > phase) {
      if (!__all(fb < 0 || vb > (unsigned)nx)) vb = __hip_atomic_load(flagp(p, fb < 0 ? FL_TMO : fb), RLX_AGENT);
      WAIT_V(0);
      BAR();
      if (tid == 0) __hip_atomic_store(flagp(p, fo), (unsigned)prev + 1u, RLX_AGENT);
    }
    CYC(c2);
    if (!have) {
      wait_prog(p, fb, seg + 1, vb, 1);
      __builtin_amdgcn_fence(__ATOMIC_ACQUIRE, "wavefront");
      loadkvm(seg);
    }
    CYC(c3);
    if (nx < NSEG && !__all(fa < 0 || va > (unsigned)nx)) va = __hip_atomic_load(flagp(p, fa < 0 ? FL_TMO : fa), RLX_AGENT);
    __builtin_amdgcn_sched_barrier(0);
#pragma unroll
    for (int ks = 0; ks < 4; ++ks) Tm = mfma32(Km[ks], Q[ks], Tm);
#pragma unroll
    for (int r = 0; r < 4; ++r) mx = fmaxf(mx, Tm[r]);
    mx = xlane32_max(xlane16_max(mx));
    const float mb = -mx * cexp;
    f32x4 p0, p1, pm;
#pragma unroll
    for (int r = 0; r < 4; ++r) {
      p0[r] = __builtin_amdgcn_exp2f(__builtin_fmaf(T0[r], cexp, mb));
      p1[r] = __builtin_amdgcn_exp2f(__builtin_fmaf(T1[r], cexp, mb));
      pm[r] = __builtin_amdgcn_exp2f(__builtin_fmaf(Tm[r], cexp, mb));
    }
    const u32x4 Pt = pk8(p0, p1);
    const u32x4 Pa = {pk_bf16(pm[0], pm[1]), pk_bf16(pm[2], pm[3]), 0u, 0u};
    const u32x4 Pb = {0u, 0u, Pa.x, Pa.y};
    f32x4 sm = mfma32(ones, Pt, z); sm = mfma32(ones_m, Pa, sm);
    f32x4 o[8];
#pragma unroll
    for (int ft = 0; ft < 8; ++ft) o[ft] = mfma32(V[ft], Pt, z);
#pragma unroll
    for (int k4 = 0; k4 < 4; ++k4) { o[2 * k4] = mfma32(Vm[k4], Pa, o[2 * k4]); o[2 * k4 + 1] = mfma32(Vm[k4], Pb, o[2 * k4 + 1]); }
    const float rinv = __builtin_amdgcn_rcpf(sm[0]);
    LAS u32x4* O = (LAS u32x4*)(L + 98304) + lane;
#pragma unroll
    for (int k4 = 0; k4 < 4; ++k4) O[(qt * 8 + hh * 4 + k4) * 64] = pk8(o[2 * k4] * rinv, o[2 * k4 + 1] * rinv);
    const bool next = nx < NSEG, more = next && __all(fa < 0 || va > (unsigned)nx);
    __builtin_amdgcn_fence(__ATOMIC_ACQUIRE, "wavefront");
    have = next && __all(fb < 0 || vb > (unsigned)nx);
    if (have) loadkvm(nx);
    const unsigned po = pin_s(spread(p, PO_OFF, (seg * 16 + pr) * 32768) + w * 256);
    desc_t dnx = dws;
    dnx.z = pin_s(more ? WS_END : 0u);
    unsigned fso[3], flo[3];
    fso[0] = pin_s(spread(p, QF_OFF, ((nx & 63) * H_ + 2 * pr) * 8192 + 4 * w * 1024)); fso[1] = pin_s(fso[0] - QF_OFF + KF_OFF); fso[2] = pin_s(fso[0] - QF_OFF + VF_OFF);
    flo[0] = pin_s((slot ^ 1) * 49152 + 4 * w * 1024); flo[1] = pin_s(flo[0] + 16384); flo[2] = pin_s(flo[0] + 32768);
    WAIT_L(0);
    BAR();
    CYC(c4);
    u32x4 of[2][8];
    of[0][0] = O[0]; of[0][1] = O[64];
    u32x4 rb[4];
    f32x4 y[2][4];
    u32x4 pk;
    LAS unsigned char* T = L + 114752 + w * 8704;
#pragma unroll
    for (int gi = 0; gi <= 5; ++gi) {
#pragma unroll
      for (int ks = 0; ks < 8; ++ks) {
#pragma unroll
        for (int m = 0; m < 4; ++m) {
          if (gi < 4) y[gi & 1][m] = mfma32(Wf[(gi & 1) * 4 + m][ks], of[gi >> 1][ks], ks ? y[gi & 1][m] : z);
          __builtin_amdgcn_sched_barrier(0);
          if (gi >= 1 && gi <= 4 && ks >= 1 && ks <= 4) {
            const int pg = gi - 1;
            if ((m & 1) == 0) {
              const int e = (ks - 1) * 2 + (m >> 1), q4 = e & 3;
              const f32x4 src = y[pg & 1][(e >> 2) * 2 + (q4 >> 1)];
              pk[q4] = pk_bf16(src[(q4 & 1) * 2], src[(q4 & 1) * 2 + 1]);
            }
            if ((ks == 2 || ks == 4) && m == 3) *(LAS u32x4*)(T + ((pg >> 1) * 16 + c) * 272 + ((pg & 1) * 2 + (ks >> 1) - 1) * 64 + g * 16) = pk;
          }
          if (gi == 0 && ks < 7 && (m & 1) == 0) { const int i = 2 + ks * 2 + (m >> 1); of[i >> 3][i & 7] = O[i * 64]; }
          if ((gi == 2 || gi == 4) && ks == 6) rb[m] = *(LAS u32x4*)(T + (((gi - 2) >> 1) * 16 + m * 4 + g) * 272 + c * 16);
          if ((gi == 3 || gi == 5) && ks < 4 && m == 1) {
            const int row = ((gi - 3) >> 1) * 16 + ks * 4 + g;
            st_sc1(rb[ks], rws, (row >> 3) * 16384 + (row & 7) * 1024 + c * 16, po);
          }
          if (gi < 3 && gi * 8 + ks >= 2 && gi * 8 + ks < 14 && m == 1) { const int i = gi * 8 + ks - 2; dma16q<true>(i & 3, dnx, lane * 16, fso[i >> 2], flo[i >> 2]); }
          __builtin_amdgcn_sched_barrier(0);
        }
      }
    }
    CYC(c45);
    if (next && !more) {
      WAIT_V(0);
      late = 1;
    } else WAIT_V(8);
    if (__builtin_amdgcn_readfirstlane(late)) *lateflag = seg + 1;
    WAIT_L(0);
    BAR();
    if (*lateflag == seg + 1) {
      WAIT_V(0);
      BAR();
      if (tid == 0) __hip_atomic_store(flagp(p, fo), (unsigned)seg + 1u, RLX_AGENT);
      if (late) {
        wait_prog(p, fa, nx + 1, va, 4);
#pragma unroll
        for (int i = 0; i < 12; ++i) fetch1(nx, slot ^ 1, i);
        WAIT_V(0);
      }
      BAR();
    }
    CYC(c5);
    if (seg >= 32) { ACC(0, 2 * (c1 - c0)); ACC(1, 2 * (c2 - c1)); ACC(2, 2 * (c3 - c2)); ACC(3, 2 * (c4 - c3)); ACC(4, 2 * (c45 - c4)); ACC(5, 2 * (c5 - c45)); ACC(6, have ? 64 : 0); ACC(7, more ? 64 : 0); }
  }
  WAIT_V(0);
  BAR();
  if (tid == 0) __hip_atomic_store(flagp(p, fo), (unsigned)NSEG, RLX_AGENT);
  ACCOUT;
}

__device__ __forceinline__ float dot_lo(unsigned x, float acc) { return __builtin_amdgcn_fdot2_f32_bf16(__builtin_bit_cast(bf16x2, x), __builtin_bit_cast(bf16x2, 0x00003F80u), acc, false); }
__device__ __forceinline__ float dot_hi(unsigned x, float acc) { return __builtin_amdgcn_fdot2_f32_bf16(__builtin_bit_cast(bf16x2, x), __builtin_bit_cast(bf16x2, 0x3F800000u), acc, false); }
__device__ __forceinline__ u32x4 round8(float (&f)[8]) {
  const u32x4 o = {pk_bf16(f[0], f[1]), pk_bf16(f[2], f[3]), pk_bf16(f[4], f[5]), pk_bf16(f[6], f[7])};
  f[0] = bflo(o.x); f[1] = bfhi(o.x); f[2] = bflo(o.y); f[3] = bfhi(o.y); f[4] = bflo(o.z); f[5] = bfhi(o.z); f[6] = bflo(o.w); f[7] = bfhi(o.w);
  return o;
}
__device__ __forceinline__ void r_role(const Params& p, int j) {
  const int tid = threadIdx.x, lane = tid & 63, w = __builtin_amdgcn_readfirstlane(tid >> 6);
  const rsrc_t rws = mk_rsrc(p.ws, WS_END);
  const int tok = 2 * j + (w & 1);
  const int fi = lane < 32 ? FL_AO + lane : (lane < 48 ? FL_AC + lane - 32 : -1);
  unsigned fv = 0;
  for (int seg = w >> 1; seg < NSEG; seg += 2) {
    const int row = seg * 32 + tok;
    const u32x4 xv = *(const u32x4*)(p.x + (size_t)row * D_ + lane * 8);
    const int from = seg < SEG3 ? (seg & 1) : (seg - SEG3 + 2) % 3;
    wait_prog(p, (lane >> 4) == from ? fi : -1, seg + 1, fv, 1);
    fv = 0;
    u32x4 v[16];
    const unsigned pob = spread(p, PO_OFF, seg * 16 * 32768 + tok * 1024);
#pragma unroll
    for (int k = 0; k < 16; ++k) v[k] = ld_sc1(rws, lane * 16, pob + k * 65536);
    float f[8] = {0.f, 0.f, 0.f, 0.f, 0.f, 0.f, 0.f, 0.f};
#pragma unroll
    for (int k = 0; k < 16; ++k) {
      f[0] += bflo(v[k].x); f[1] += bfhi(v[k].x); f[2] += bflo(v[k].y); f[3] += bfhi(v[k].y);
      f[4] += bflo(v[k].z); f[5] += bfhi(v[k].z); f[6] += bflo(v[k].w); f[7] += bfhi(v[k].w);
    }
    round8(f);
    f[0] += bflo(xv.x); f[1] += bfhi(xv.x); f[2] += bflo(xv.y); f[3] += bfhi(xv.y); f[4] += bflo(xv.z); f[5] += bfhi(xv.z); f[6] += bflo(xv.w); f[7] += bfhi(xv.w);
    st_sc1(round8(f), rws, lane * 16, spread(p, HB_OFF, row * 1024));
    ln8(f); round8(f); ln8(f);
    st_sc1(round8(f), rws, lane * 16, spread(p, HN_OFF, row * 1024));
    WAIT_V(0);
    if (lane == 0) __hip_atomic_store(flagp(p, FL_R + (w >> 1) * 32 + tok), (unsigned)(seg >> 1) + 1u, RLX_AGENT);
  }
}

__device__ __forceinline__ unsigned gelu_pair(float a, float b) {
  const f32x2 x = {a, b};
  const f32x2 e = x * __builtin_elementwise_fma(x * x, f32x2{-0.10294324f, -0.10294324f}, f32x2{-2.3022082f, -2.3022082f});
  const f32x2 d = f32x2{__builtin_amdgcn_exp2f(e.x), __builtin_amdgcn_exp2f(e.y)} + f32x2{1.0f, 1.0f};
  const f32x2 r = x * f32x2{__builtin_amdgcn_rcpf(d.x), __builtin_amdgcn_rcpf(d.y)};
  return pk_bf16(r.x, r.y);
}
__device__ __forceinline__ void m1_role(const Params& p, int tile, int quiet, LAS unsigned char* L) {
  const int tid = threadIdx.x, lane = tid & 63, w = __builtin_amdgcn_readfirstlane(tid >> 6);
  const int g = lane >> 4, q = (lane >> 2) & 3, pp = lane & 3;
  const int dq = tile & 1, fb = tile >> 1;
  const rsrc_t rws = mk_rsrc(p.ws, WS_END);
  const desc_t dws = mk_desc(p.ws, WS_END), rw1 = mk_desc(p.w1, S_ * F_ * 2);
  constexpr int SLOT = 20480, NS = 6;
  auto fetch_w = [&](int seg) {
    const int row = 8 * w + (lane >> 3);
    const int sw = ((row >> 1) & 1) * 2 + ((row >> 3) & 1) * 4;
    dma16<false>(rw1, (unsigned)(lane >> 3) * (F_ * 2) + (unsigned)(((lane & 7) ^ sw) * 16), (unsigned)(seg * 32 + 8 * w) * (F_ * 2) + fb * 128, (seg % NS) * SLOT + 16384 + w * 1024);
  };
  auto fetch_h = [&](int seg) {
#pragma unroll
    for (int i = 0; i < 4; ++i) {
      const int phi = ((2 * i + (lane >> 5)) & 3) | ((w & 1) << 2);
      dma16<true>(dws, (unsigned)(lane >> 5) * 1024 + (unsigned)(((lane & 31) ^ (phi << 1)) * 16), spread(p, HN_OFF, (seg * 32 + 8 * w) * 1024) + 2 * i * 1024 + dq * 512, (seg % NS) * SLOT + (8 * w + 2 * i) * 512);
    }
  };
  f32x4 acc[4][4];
#pragma unroll
  for (int a = 0; a < 4; ++a)
#pragma unroll
    for (int b = 0; b < 4; ++b) acc[a][b] = f32x4{0.f, 0.f, 0.f, 0.f};
  float bias[2][8];
#pragma unroll
  for (int k2 = 0; k2 < 2; ++k2) {
    const u32x2 ba = *(const u32x2*)(p.b1 + fb * 64 + k2 * 32 + 4 * g), bb = *(const u32x2*)(p.b1 + fb * 64 + k2 * 32 + 16 + 4 * g);
    bias[k2][0] = bflo(ba.x); bias[k2][1] = bfhi(ba.x); bias[k2][2] = bflo(ba.y); bias[k2][3] = bfhi(ba.y);
    bias[k2][4] = bflo(bb.x); bias[k2][5] = bfhi(bb.x); bias[k2][6] = bflo(bb.y); bias[k2][7] = bfhi(bb.y);
  }
#pragma unroll
  for (int a = 0; a < 4; ++a)
#pragma unroll
    for (int b = 0; b < 4; ++b)
#pragma unroll
      for (int r = 0; r < 4; ++r) acc[a][b][r] = bias[a >> 1][(a & 1) * 4 + r];
  volatile LAS int* rdy = (volatile LAS int*)(L + NS * SLOT);
  unsigned fv = 0;
  int issued = 0, ready = 0;
  fetch_w(0); fetch_w(1); fetch_w(2); fetch_w(3);
  for (int seg = 0; seg < NSEG; ++seg) {
    if (ready <= seg && seg >= quiet) {
      unsigned fo = 0;
      wait_prog(p, lane < 8 ? FL_R + (seg & 1) * 32 + 8 * w + lane : -1, (seg >> 1) + 1, fo, 1);
      ready = seg + 1;
    }
    if (ready <= seg) {
      if (w == 0) {
        wait_prog(p, FL_R + lane, (lane >> 5) == (seg & 1) ? (seg >> 1) + 1 : 0, fv, seg < quiet - 3 ? 32 : 4);
        int r = seg + 1;
        while (r < NSEG && __all((lane >> 5) != (r & 1) || fv > (unsigned)(r >> 1))) ++r;
        rdy[seg & 1] = r;
        WAIT_L(0);
      }
      BAR();
      ready = __builtin_amdgcn_readfirstlane(rdy[seg & 1]);
    }
    while (issued < ready && issued < seg + 4) { fetch_h(issued); ++issued; }
    const int ahead = issued - seg - 1;
    if (ahead >= 3) WAIT_V(12); else if (ahead == 2) WAIT_V(8); else if (ahead == 1) WAIT_V(4); else WAIT_V(0);
    BAR();
    if (seg + 4 < NSEG) fetch_w(seg + 4);
    if (seg < 56 && ahead >= 1) __builtin_amdgcn_s_sleep(3);
    LAS unsigned char* base = L + (seg % NS) * SLOT;
    u32x4 B[4], A[4];
    const int phi = q | ((g & 1) << 2);
    const int sw = ((q >> 1) & 1) * 2 + (g & 1) * 4;
#pragma unroll
    for (int t = 0; t < 4; ++t) {
      LAS unsigned char* a = base + 16384 + (8 * g + q) * 128 + (((2 * t + (pp >> 1)) ^ sw) * 16) + (pp & 1) * 8;
      A[t] = cat(lds_tr(a), lds_tr(a + 512));
      LAS unsigned char* b = base + (8 * g + q) * 512 + (((8 * w + 2 * t + (pp >> 1)) ^ (phi << 1)) * 16) + (pp & 1) * 8;
      B[t] = cat(lds_tr(b), lds_tr(b + 2048));
    }
#pragma unroll
    for (int ft = 0; ft < 4; ++ft)
#pragma unroll
      for (int dt = 0; dt < 4; ++dt) acc[ft][dt] = mfma32(A[ft], B[dt], acc[ft][dt]);
  }
#pragma unroll
  for (int k2 = 0; k2 < 2; ++k2)
#pragma unroll
    for (int dt = 0; dt < 4; ++dt) {
      const f32x4 a = acc[2 * k2][dt], b = acc[2 * k2 + 1][dt];
      st_sc1(u32x4{gelu_pair(a[0], a[1]), gelu_pair(a[2], a[3]), gelu_pair(b[0], b[1]), gelu_pair(b[2], b[3])}, rws, lane * 16, G_OFF + ((dq * 16 + 4 * w + dt) * 56 + fb * 2 + k2) * 1024);
    }
  WAIT_V(0);
  BAR();
  if (tid == 0) __hip_atomic_store(flagp(p, FL_G + tile), 1u, RLX_AGENT);
}

__device__ __forceinline__ void m2_role(const Params& p, int db, int sb, int m1tile, LAS unsigned char* L) {
  const int tid = threadIdx.x, lane = tid & 63, w = __builtin_amdgcn_readfirstlane(tid >> 6);
  const int g = lane >> 4, c = lane & 15, q = (lane >> 2) & 3, pp = lane & 3;
  const desc_t rw2 = mk_desc(p.w2, F_ * S_ * 2);
  const rsrc_t rws = mk_rsrc(p.ws, WS_END);
  TS(4);
  for (int i = 0; i < m1tile; ++i) __builtin_amdgcn_s_sleep(4);
  const unsigned lw = w * 36864;
  auto fetchw = [&](int ks) {
#pragma unroll
    for (int i = 0; i < 4; ++i) {
      const int row = 8 * i + (lane >> 3);
      const int sw = ((row >> 1) & 1) * 2 + ((row >> 2) & 1) * 4;
      dma16<false>(rw2, (unsigned)(lane >> 3) * (S_ * 2) + (unsigned)(((lane & 7) ^ sw) * 16), (unsigned)(448 * w + 32 * ks + 8 * i) * (S_ * 2) + sb * 128, lw + (ks % 9) * 4096 + i * 1024);
    }
  };
  u32x4 B[14][4];
#pragma unroll
  for (int ks = 0; ks < 9; ++ks) fetchw(ks);
  const int sw = ((q >> 1) & 1) * 2 + (g & 1) * 4;
#pragma unroll
  for (int ks = 0; ks < 14; ++ks) {
    switch (ks) { case 0: case 1: case 2: case 3: case 4: case 5: WAIT_V(32); break; case 6: WAIT_V(28); break; case 7: WAIT_V(24); break; case 8: WAIT_V(20); break;
      case 9: WAIT_V(16); break; case 10: WAIT_V(12); break; case 11: WAIT_V(8); break; case 12: WAIT_V(4); break; default: WAIT_V(0); }
#pragma unroll
    for (int t = 0; t < 4; ++t) {
      LAS unsigned char* a = L + lw + (ks % 9) * 4096 + (4 * g + q) * 128 + (((2 * t + (pp >> 1)) ^ sw) * 16) + (pp & 1) * 8;
      B[ks][t] = cat(lds_tr(a), lds_tr(a + 2048));
    }
    WAIT_L(0);
    if (ks + 9 < 14) fetchw(ks + 9);
  }
#pragma unroll
  for (int ks = 3; ks < 14; ++ks)
#pragma unroll
    for (int t = 0; t < 4; ++t) asm volatile("" : "+a"(B[ks][t]));
  if (m1tile >= 0) { BAR(); m1_role(p, m1tile, 60, L); TS(2); }
  const float b2 = bflo(p.b2[sb * 64 + (tid >> 2)]);
  TS(3);
  unsigned fv = 0, fq = 0;
  wait_prog(p, lane == 0 ? FL_R : -1, 30, fq, 32);
  if (m1tile == -2 && w > 0) touch_role(p, w);
  wait_prog(p, lane < 7 ? FL_G + 2 * (7 * w + lane) + (db >> 2) : -1, 1, fv, 1);
  TS(6);
  constexpr int WIN = 40;
  u32x4 a[WIN];
  auto ldg = [&](int i) { return ld_sc1(rws, lane * 16, G_OFF + ((db * 4 + (i & 3)) * 56 + 14 * w + (i >> 2)) * 1024); };
#pragma unroll
  for (int i = 0; i < WIN; ++i) a[i] = ldg(i);
  __builtin_amdgcn_sched_barrier(0);
  f32x4 acc[4][4];
  const int so = sb * 64 + (tid >> 2), dc = db * 64 + (tid & 3) * 16;
  u32x4 hv[2];
#pragma unroll
  for (int i = 0; i < 56; ++i) {
    const u32x4 cur = a[i % WIN];
#pragma unroll
    for (int t = 0; t < 4; ++t) {
      acc[i & 3][t] = mfma32(cur, B[i >> 2][t], i < 4 ? f32x4{0.f, 0.f, 0.f, 0.f} : acc[i & 3][t]);
      if (t == 3 && i + WIN < 56) a[i % WIN] = ldg(i + WIN);
    }
    if (i == 16 || i == 17) hv[i - 16] = ld_sc1(rws, spread(p, HB_OFF, so * 1024 + dc * 2) + (i - 16) * 16, 0);
    __builtin_amdgcn_sched_barrier(0);
  }
  TS(7);
  BAR();
#pragma unroll
  for (int t = 0; t < 4; ++t)
#pragma unroll
    for (int dt = 0; dt < 4; ++dt) *(LAS f32x4*)(L + w * 17408 + (16 * t + c) * 272 + (16 * dt + 4 * g) * 4) = acc[dt][t];
  WAIT_L(0);
  BAR();
  u32x4 o[2];
#pragma unroll
  for (int k = 0; k < 4; ++k) {
    f32x4 y = {b2, b2, b2, b2};
#pragma unroll
    for (int ww = 0; ww < 4; ++ww) y += *(LAS f32x4*)(L + ww * 17408 + (tid >> 2) * 272 + ((tid & 3) * 16 + 4 * k) * 4);
    const unsigned y0 = pk_bf16(y[0], y[1]), y1 = pk_bf16(y[2], y[3]);
    const unsigned h0 = hv[k >> 1][(k & 1) * 2], h1 = hv[k >> 1][(k & 1) * 2 + 1];
    o[k >> 1][(k & 1) * 2] = pk_bf16(bflo(h0) + bflo(y0), bfhi(h0) + bfhi(y0));
    o[k >> 1][(k & 1) * 2 + 1] = pk_bf16(bflo(h1) + bflo(y1), bfhi(h1) + bfhi(y1));
  }
  *(u32x4*)(p.out + (size_t)so * D_ + dc) = o[0];
  *(u32x4*)(p.out + (size_t)so * D_ + dc + 8) = o[1];
  TS(5);
}
__device__ __forceinline__ void chain_role(const Params& p, int cid, unsigned long long tag) {
  const int w = __builtin_amdgcn_readfirstlane(threadIdx.x >> 6);
  asm volatile(CHAIN_ASM :: "{s[4:5]}"(tag << 16), "{s[6:7]}"(p.Wq), "{s[8:9]}"(p.Wk), "{s[10:11]}"(p.Wv), "{s[12:13]}"(p.Wo), "{s[14:15]}"(p.ws), "{s18}"(cid), "{s19}"(w), "{s20}"(p.stg), "{s21}"(p.ca), "{s[16:17]}"(p.dbg), "{s22}"(0), "{s[24:25]}"(__builtin_amdgcn_kernarg_segment_ptr()), "{s23}"(blockIdx.x) : CHAIN_CLOBBERS);
  __builtin_unreachable();
}
__device__ __forceinline__ void red_role(Params& p, int r, unsigned long long tag, unsigned t0, LAS unsigned char* L) {
  const int w = __builtin_amdgcn_readfirstlane(threadIdx.x >> 6);
  const unsigned sel = __builtin_amdgcn_readfirstlane(setup_role(p, r, tag, L));
  if (w == 0) init_wait(p, tag);
  asm volatile(RED_ASM :: "{s[14:15]}"(p.ws), "{s18}"(r), "{s19}"(sel), "{s20}"(p.ca), "{s21}"(p.cr + (r >> 5) * p.stg + (r >> 3) * p.stg2), "{s[16:17]}"(p.dbg), "{s22}"(p.cs), "{s[24:25]}"(__builtin_amdgcn_kernarg_segment_ptr()), "{s23}"(blockIdx.x), "{s4}"(w) : CHAIN_CLOBBERS);
  __builtin_unreachable();
}

extern "C" __global__ void __launch_bounds__(256, 1) k_main(Params p) {
  extern __shared__ __attribute__((aligned(16))) unsigned char smem[];
  LAS unsigned char* L = (LAS unsigned char*)smem;
  const int b = blockIdx.x, gx = b & 7, j = b >> 3;
  int m1tile = -1;
  unsigned long long tag = 0;
  p.fsel = ~(unsigned)((b >> 8) - 1);
  if (b < 256) {
    TS(0);
    const unsigned t0 = (unsigned)__builtin_amdgcn_s_memrealtime();
    const u32x2 ep = *(const u32x2*)(p.ws + EPOCH_OFF);
    const unsigned e0 = __builtin_amdgcn_readfirstlane(ep.x), e1 = __builtin_amdgcn_readfirstlane(ep.y);
    tag = (((((unsigned long long)e1 << 32) | e0) >> 16) + 1) & 0xFFFFFFFFFFFFull;
    if (j == 26 && gx >= 4 && !(p.roles & 128)) touch_role(p, __builtin_amdgcn_readfirstlane(threadIdx.x >> 6));
    if (gx < 4) {
      if (p.roles & 2) { if (j < 16) chain_role(p, gx * 16 + j, tag); else red_role(p, (j - 16) * 4 + gx, tag, t0, L); }
    } else {
      const int x = gx - 4;
      if (j < 16) {
        const int h = x * 8 + (j & 7);
        if (p.roles & 1) { if (j < 8) qkv_role<0>(p, tag, p.Wk, h, KF_OFF, FL_K + h, L); else qkv_role<1>(p, tag, p.Wv, h, VF_OFF, FL_V + h, L); }
        TS(1);
        m1tile = j < 14 ? x * 14 + j : (j == 14 ? -2 : -1);
      } else if (j < 28) {
        int pr = x * 4 + ((j - 20) >> 1), set = (j - 20) & 1;
        if (j < 20) {
          pr = x * 4 + j - 16; set = 2;
          while ((unsigned)__builtin_amdgcn_s_memrealtime() - t0 < (p.pad >> 8)) __builtin_amdgcn_s_sleep(4);
          if (p.roles & 1) qkv_role<2>(p, tag, p.Wq, 2 * pr, QF_OFF, FL_Q + pr, L);
          TS(1);
          BAR();
        } else {
          while ((unsigned)__builtin_amdgcn_s_memrealtime() - t0 < (p.pad >> 8) + 100) __builtin_amdgcn_s_sleep(4);
        }
        if (p.roles & 4) ao_role(p, tag, t0, pr, set, L);
        TS(2);
      } else {
        ln_role(p, x * 4 + j - 28, tag, L);
        TS(6);
        bg_warm<4, 0>(p, t0, 640 + ((x * 4 + j - 28) * 4 + __builtin_amdgcn_readfirstlane(threadIdx.x >> 6)) * 4, 0);
        if (p.roles & 8) r_role(p, x * 4 + j - 28);
        TS(2);
      }
    }
  }
  if (b >= 256 && (j & 31) == 16) m1tile = -2;
  if (p.roles & 16) { BAR(); m2_role(p, (gx & 1) * 4 + (j & 3), (gx >> 1) * 8 + ((j & 31) >> 2), m1tile, L); }
  if (b == 255 && threadIdx.x == 0) *(unsigned long long*)(p.ws + EPOCH_OFF) = tag << 16;
}

#define P_ROLES 31
#define P_PAD (9 | 500 << 8)
#define P_STG 20
#define P_CA 1075
#define P_CR 155
#define P_STG2 36
#define P_CS 1150
static void launch_all(Params p, hipStream_t stream, int split) {
  if (split) {
    for (int r = 1; r <= 16; r <<= 1) { p.roles = r; hipLaunchKernelGGL(k_main, dim3(256), dim3(256), LDS_BYTES, stream, p); }
  } else {
    hipLaunchKernelGGL(k_main, dim3(256), dim3(256), LDS_BYTES, stream, p);
  }
}
static Params make_params(void* const* d_in, void* d_out, void* d_ws) {
  Params p;
  p.x = (const u16*)d_in[0]; p.mem0 = (const u16*)d_in[1]; p.Wq = (const u16*)d_in[2]; p.Wk = (const u16*)d_in[3]; p.Wv = (const u16*)d_in[4]; p.Wo = (const u16*)d_in[5];
  p.w1 = (const u16*)d_in[8]; p.b1 = (const u16*)d_in[9]; p.w2 = (const u16*)d_in[10]; p.b2 = (const u16*)d_in[11];
  p.out = (u16*)d_out;
  p.ws = (unsigned char*)(((size_t)d_ws + 0x1FFFFF) & ~(size_t)0x1FFFFF);
  p.dbg = nullptr;
  p.stg = P_STG; p.ca = P_CA; p.cr = P_CR; p.stg2 = P_STG2; p.cs = P_CS; p.roles = P_ROLES; p.fsel = 0; p.pad = P_PAD;
  return p;
}
extern "C" void kernel_launch(void* const* d_in, const int* in_sizes, int n_in, void* d_out, int out_size, void* d_ws, size_t ws_size, hipStream_t stream) {
  (void)in_sizes; (void)n_in; (void)out_size; (void)ws_size;
  (void)hipFuncSetAttribute((const void*)k_main, hipFuncAttributeMaxDynamicSharedMemorySize, LDS_BYTES);
  launch_all(make_params(d_in, d_out, d_ws), stream, 0);
}
```
